```python
import torch

def _softmax(x, axis=-1):
    return torch.softmax(x, dim=axis)
def _gelu(x):
    return torch.nn.functional.gelu(x, approximate="tanh")
def _silu(x):
    return torch.nn.functional.silu(x)
def _sigmoid(x):
    return torch.sigmoid(x)
def _layernorm(x, axis=-1, eps=1e-5):
    m = x.mean(dim=axis, keepdim=True)
    v = x.var(dim=axis, keepdim=True, unbiased=False)
    return (x - m) / torch.sqrt(v + eps)
def _scatter_add(tgt, idx, src, axis=0):
    return tgt.clone().index_add_(axis, idx, src)
def _segment_sum(src, seg, n):
    out = torch.zeros((n,) + src.shape[1:], dtype=src.dtype)
    return out.index_add_(0, seg, src)
def _pad(x, pw):
    flat = tuple(v for pair in reversed(pw) for v in pair)
    return torch.nn.functional.pad(x, flat)

kernel_name = 'gated_mix__gqa_block_cached__moe_arch_mod__decode'

def setup_inputs():

    _w = (lambda a: (lambda t: t.to(torch.bfloat16) if t.is_floating_point() else t)(torch.as_tensor(a)))
    def _A_setup():
        B, Sq, D = 1, 1, 1024
        Hq, Hkv, Sk, Dh = 32, 4, 65536, 128
        assert Hq % Hkv == 0, (Hq, Hkv)
        return {
            "x": _w((lambda *s: torch.randn(*s, dtype=torch.bfloat16) * 0.05)(B, Sq, D)),
            "k_cache": _w((lambda *s: torch.randn(*s, dtype=torch.bfloat16) * 3.2)(B, Hkv, Sk, Dh)),
            "v_cache": _w((lambda *s: torch.randn(*s, dtype=torch.bfloat16) * 1.6)(B, Hkv, Sk, Dh)),
            "wq": _w((lambda *s: torch.randn(*s, dtype=torch.bfloat16) * 3.2)(D, Hq * Dh) * (D ** -0.5)),
            "wo": _w((lambda *s: torch.randn(*s, dtype=torch.bfloat16) * 1.6)(Hq * Dh, D) * ((Hq * Dh) ** -0.5)),
        }
    _A_in = _A_setup()

    def _B_setup():
        B, S, D, F, C = 1, 1, 1024, 3584, 1
        return {
            "x": _w((lambda *s: torch.randn(*s, dtype=torch.bfloat16) * 0.05)(B, S, D)),
            "w_r": _w((lambda *s: torch.randn(*s, dtype=torch.bfloat16) * 0.05)(D)),
            "w_gate": _w((lambda *s: torch.randn(*s, dtype=torch.bfloat16) * 0.05)(D, F)),
            "w_up": _w((lambda *s: torch.randn(*s, dtype=torch.bfloat16) * 0.05)(D, F)),
            "w_down": _w((lambda *s: torch.randn(*s, dtype=torch.bfloat16) * 0.05)(F, D)),
        }
    _B_in = _B_setup()

    return {
        "A_x": _A_in["x"],
        "A_k_cache": _A_in["k_cache"],
        "A_v_cache": _A_in["v_cache"],
        "A_wq": _A_in["wq"],
        "A_wo": _A_in["wo"],
        "B_x": _B_in["x"],
        "B_w_r": _B_in["w_r"],
        "B_w_gate": _B_in["w_gate"],
        "B_w_up": _B_in["w_up"],
        "B_w_down": _B_in["w_down"],
        "gate_w": _w((lambda *s: torch.randn(*s, dtype=torch.bfloat16) * 0.05)(1024) * 16.0),
    }

def reference(A_x, A_k_cache, A_v_cache, A_wq, A_wo, B_x, B_w_r, B_w_gate, B_w_up, B_w_down, gate_w):
    def _A_ref(x, k_cache, v_cache, wq, wo):
        B, Sq, D = x.shape
        Hq, Dh = 32, 128
        Hkv = k_cache.shape[1]
        rep = Hq // Hkv
        q = (lambda x, *d: x.permute(*d))((x @ wq).reshape(B, Sq, Hq, Dh), 0, 2, 1, 3)
        k = (lambda x, n, axis: torch.repeat_interleave(x, n, dim=axis))(k_cache, rep, axis=1)
        v = (lambda x, n, axis: torch.repeat_interleave(x, n, dim=axis))(v_cache, rep, axis=1)
        scores = torch.einsum("bhqd,bhkd->bhqk", q, k) * (Dh ** -0.5)
        p = _softmax(scores, axis=-1)
        o = torch.einsum("bhqk,bhkd->bhqd", p, v)
        return (lambda x, *d: x.permute(*d))(o, 0, 2, 1, 3).reshape(B, Sq, Hq * Dh) @ wo

    def _B_ref(x, w_r, w_gate, w_up, w_down):
        B, S, D = x.shape
        C = 1
        r = x @ w_r
        idx = torch.argsort(-r, axis=-1)[:, :C]
        idx_e = (lambda x, n, axis: torch.repeat_interleave(x, n, dim=axis))(idx.reshape(B, C, 1), D, axis=2)
        x_sel = (lambda x, i, axis: torch.take_along_dim(x, i, dim=axis))(x, idx_e, axis=1)
        g = _silu(x_sel @ w_gate)
        u = x_sel @ w_up
        y = (g * u) @ w_down
        r_sel = _sigmoid((lambda x, i, axis: torch.take_along_dim(x, i, dim=axis))(r, idx, axis=1))
        y = y * r_sel.reshape(B, C, 1)
        b_off = (torch.arange(B) * S).reshape(B, 1)
        flat_idx = (idx + b_off).reshape(B * C)
        zero = (x * 0.0).reshape(B * S, D)
        upd = _scatter_add(zero, flat_idx, y.reshape(B * C, D), axis=0)
        return x + upd.reshape(B, S, D)

    x = A_x
    g = _sigmoid((x * gate_w).sum(-1, keepdims=True))
    a = _A_ref(x, A_k_cache, A_v_cache, A_wq, A_wo)
    b = _B_ref(x, B_w_r, B_w_gate, B_w_up, B_w_down)
    return g * a + (1 - g) * b
```

```cpp
#include <hip/hip_runtime.h>
#include <hip/hip_bf16.h>
#include <cstdint>

typedef unsigned short u16;
typedef unsigned int u32;
typedef u32 v4u __attribute__((ext_vector_type(4)));
typedef float f32x4 __attribute__((ext_vector_type(4)));
typedef short bf16x8 __attribute__((ext_vector_type(8)));

constexpr int D = 1024, HQ = 32, HKV = 4, SK = 65536, DH = 128, F = 3584;
constexpr int NSPLIT = 64;
constexpr int KPS = SK / NSPLIT;

__device__ __forceinline__ float bf2f(u32 b) { return __builtin_bit_cast(float, b << 16); }
__device__ __forceinline__ float bflo(u32 w) { return __builtin_bit_cast(float, w << 16); }
__device__ __forceinline__ float bfhi(u32 w) { return __builtin_bit_cast(float, w & 0xffff0000u); }
__device__ __forceinline__ u32 f2bf(float f) { u32 u = __builtin_bit_cast(u32, f); u += 0x7FFFu + ((u >> 16) & 1u); return u >> 16; }

constexpr size_t WS_Q = 0;
constexpr size_t WS_H = WS_Q + 4096;
constexpr size_t WS_PART = WS_H + 4096;
constexpr size_t WS_ML = WS_PART + 32 * 64 * 128;
constexpr size_t WS_O = WS_ML + 32 * 64 * 2;
constexpr size_t WS_OUTP = WS_O + 4096;

__global__ void __launch_bounds__(256) k1_proj(const u16* __restrict__ x, const u16* __restrict__ wq, const u16* __restrict__ wg, const u16* __restrict__ wu, float* __restrict__ ws) {
    __shared__ float xs[D];
    __shared__ float red[2][32][64];
    const int tid = threadIdx.x;
    for (int i = tid; i < D; i += 256) xs[i] = bf2f(x[i]);
    __syncthreads();
    const int b = blockIdx.x;
    const int c8 = tid & 7, r = tid >> 3;
    if (b < 64) {
        const int col0 = b * 64 + c8 * 8;
        float acc[8] = {0};
        for (int row = r; row < D; row += 32) {
            v4u w = *(const v4u*)(wq + (size_t)row * 4096 + col0);
            float xv = xs[row];
            for (int k = 0; k < 4; ++k) { acc[2*k] += xv * bflo(w[k]); acc[2*k+1] += xv * bfhi(w[k]); }
        }
        for (int k = 0; k < 8; ++k) red[0][r][c8 * 8 + k] = acc[k];
        __syncthreads();
        if (tid < 64) { float s = 0; for (int i = 0; i < 32; ++i) s += red[0][i][tid]; ws[WS_Q + b * 64 + tid] = bf2f(f2bf(s)); }
    } else {
        const int bb = b - 64;
        const int col0 = bb * 64 + c8 * 8;
        float ag[8] = {0}, au[8] = {0};
        for (int row = r; row < D; row += 32) {
            v4u w = *(const v4u*)(wg + (size_t)row * F + col0);
            v4u u = *(const v4u*)(wu + (size_t)row * F + col0);
            float xv = xs[row];
            for (int k = 0; k < 4; ++k) { ag[2*k] += xv * bflo(w[k]); ag[2*k+1] += xv * bfhi(w[k]); au[2*k] += xv * bflo(u[k]); au[2*k+1] += xv * bfhi(u[k]); }
        }
        for (int k = 0; k < 8; ++k) { red[0][r][c8 * 8 + k] = ag[k]; red[1][r][c8 * 8 + k] = au[k]; }
        __syncthreads();
        if (tid < 64) { float g = 0, u = 0; for (int i = 0; i < 32; ++i) { g += red[0][i][tid]; u += red[1][i][tid]; }
            float sg = g / (1.f + __expf(-g)); ws[WS_H + bb * 64 + tid] = sg * u; }
    }
}

__global__ void __launch_bounds__(256) k2_attn(const u16* __restrict__ kc, const u16* __restrict__ vc, float* __restrict__ ws) {
    __shared__ float qs[8][128];
    __shared__ float sc[8][KPS];
    __shared__ float red[8][256];
    __shared__ float mx[8], ls[8];
    const int tid = threadIdx.x, kvh = blockIdx.x / NSPLIT, sp = blockIdx.x % NSPLIT;
    for (int i = tid; i < 1024; i += 256) qs[i >> 7][i & 127] = ws[WS_Q + kvh * 1024 + i];
    __syncthreads();
    const u16* kb = kc + ((size_t)kvh * SK + (size_t)sp * KPS) * DH;
    const u16* vb = vc + ((size_t)kvh * SK + (size_t)sp * KPS) * DH;
    for (int key = tid; key < KPS; key += 256) {
        float s[8] = {0};
        for (int c = 0; c < 16; ++c) {
            v4u w = *(const v4u*)(kb + (size_t)key * DH + c * 8);
            float f[8]; for (int k = 0; k < 4; ++k) { f[2*k] = bflo(w[k]); f[2*k+1] = bfhi(w[k]); }
            for (int h = 0; h < 8; ++h) for (int k = 0; k < 8; ++k) s[h] += f[k] * qs[h][c * 8 + k];
        }
        for (int h = 0; h < 8; ++h) sc[h][key] = s[h] * 0.08838834764831845f;
    }
    __syncthreads();
    for (int h = 0; h < 8; ++h) { float m = -INFINITY; for (int key = tid; key < KPS; key += 256) m = fmaxf(m, sc[h][key]); red[h][tid] = m; }
    __syncthreads();
    if (tid < 8) { float m = -INFINITY; for (int i = 0; i < 256; ++i) m = fmaxf(m, red[tid][i]); mx[tid] = m; }
    __syncthreads();
    for (int h = 0; h < 8; ++h) { float l = 0; for (int key = tid; key < KPS; key += 256) { float p = __expf(sc[h][key] - mx[h]); sc[h][key] = p; l += p; } red[h][tid] = l; }
    __syncthreads();
    if (tid < 8) { float l = 0; for (int i = 0; i < 256; ++i) l += red[tid][i]; ls[tid] = l; }
    __syncthreads();
    const int dp = tid & 63, kq = tid >> 6;
    float a0[8] = {0}, a1[8] = {0};
    for (int key = kq; key < KPS; key += 4) {
        u32 w = *(const u32*)(vb + (size_t)key * DH + dp * 2);
        float v0 = bflo(w), v1 = bfhi(w);
        for (int h = 0; h < 8; ++h) { float p = sc[h][key]; a0[h] += p * v0; a1[h] += p * v1; }
    }
    __syncthreads();
    float* r2 = &sc[0][0];
    for (int h = 0; h < 8; ++h) { r2[(kq * 8 + h) * 128 + dp * 2] = a0[h]; r2[(kq * 8 + h) * 128 + dp * 2 + 1] = a1[h]; }
    __syncthreads();
    for (int i = tid; i < 1024; i += 256) {
        int h = i >> 7, d = i & 127;
        float s = r2[(0 * 8 + h) * 128 + d] + r2[(1 * 8 + h) * 128 + d] + r2[(2 * 8 + h) * 128 + d] + r2[(3 * 8 + h) * 128 + d];
        ws[WS_PART + ((size_t)(kvh * 8 + h) * NSPLIT + sp) * 128 + d] = s;
    }
    if (tid < 8) { ws[WS_ML + ((size_t)(kvh * 8 + tid) * NSPLIT + sp) * 2] = mx[tid]; ws[WS_ML + ((size_t)(kvh * 8 + tid) * NSPLIT + sp) * 2 + 1] = ls[tid]; }
}

__global__ void __launch_bounds__(256) k3_out(const u16* __restrict__ x, const u16* __restrict__ wo, const u16* __restrict__ wd, const u16* __restrict__ wr, const u16* __restrict__ gw, float* __restrict__ ws) {
    __shared__ float o[16], hh[14], wgt[64], red[256], red2[256];
    __shared__ float sg[2];
    const int tid = threadIdx.x, j = blockIdx.x, h = j >> 3, d0 = (j & 7) * 16;
    { float a = 0, r = 0; for (int i = tid; i < D; i += 256) { float xv = bf2f(x[i]); a += bf2f(f2bf(xv * bf2f(gw[i]))); r += xv * bf2f(wr[i]); } red[tid] = a; red2[tid] = r; }
    __syncthreads();
    if (tid == 0) { float a = 0, r = 0; for (int i = 0; i < 256; ++i) { a += red[i]; r += red2[i]; }
        sg[0] = 1.f / (1.f + __expf(-a)); sg[1] = 1.f / (1.f + __expf(-r)); }
    if (tid < 64) {
        float m = ws[WS_ML + ((size_t)h * NSPLIT + tid) * 2], l = ws[WS_ML + ((size_t)h * NSPLIT + tid) * 2 + 1];
        float M = m; for (int o_ = 32; o_ >= 1; o_ >>= 1) M = fmaxf(M, __shfl_xor(M, o_));
        float w = __expf(m - M); float L = w * l; for (int o_ = 32; o_ >= 1; o_ >>= 1) L += __shfl_xor(L, o_);
        wgt[tid] = w / L;
    }
    if (tid >= 64 && tid < 78) hh[tid - 64] = ws[WS_H + j * 14 + tid - 64];
    __syncthreads();
    if (tid < 16) { float s = 0; for (int sp = 0; sp < NSPLIT; ++sp) s += wgt[sp] * ws[WS_PART + ((size_t)h * NSPLIT + sp) * 128 + d0 + tid]; o[tid] = s; }
    __syncthreads();
    const float g = sg[0], sr = sg[1];
    float acc[4] = {0}, accy[4] = {0};
    for (int r = 0; r < 16; ++r) { uint2 w = *(const uint2*)(wo + (size_t)(j * 16 + r) * D + tid * 4); float ov = o[r];
        acc[0] += ov * bflo(w.x); acc[1] += ov * bfhi(w.x); acc[2] += ov * bflo(w.y); acc[3] += ov * bfhi(w.y); }
    for (int r = 0; r < 14; ++r) { uint2 w = *(const uint2*)(wd + (size_t)(j * 14 + r) * D + tid * 4); float hv = hh[r];
        accy[0] += hv * bflo(w.x); accy[1] += hv * bfhi(w.x); accy[2] += hv * bflo(w.y); accy[3] += hv * bfhi(w.y); }
    for (int k = 0; k < 4; ++k) ws[WS_OUTP + (size_t)j * D + tid * 4 + k] = g * acc[k] + (1.f - g) * sr * accy[k];
}

__global__ void __launch_bounds__(256) k4_fin(const u16* __restrict__ x, const u16* __restrict__ gw, const float* __restrict__ ws, u16* __restrict__ out) {
    __shared__ float red[256];
    __shared__ float r4[4][64];
    const int tid = threadIdx.x;
    { float a = 0; for (int i = tid; i < D; i += 256) { float xv = bf2f(x[i]); a += bf2f(f2bf(xv * bf2f(gw[i]))); } red[tid] = a; }
    __syncthreads();
    float a = 0; for (int i = 0; i < 256; ++i) a += red[i];
    const float g = 1.f / (1.f + __expf(-a));
    const int c = blockIdx.x * 64 + (tid & 63), rg = tid >> 6;
    float s = 0; for (int r = rg; r < 256; r += 4) s += ws[WS_OUTP + (size_t)r * D + c];
    r4[rg][tid & 63] = s;
    __syncthreads();
    if (tid < 64) { float t = r4[0][tid] + r4[1][tid] + r4[2][tid] + r4[3][tid]; out[c] = (u16)f2bf(t + (1.f - g) * bf2f(x[c])); }
}

extern "C" void kernel_launch(void* const* d_in, const int* in_sizes, int n_in, void* d_out, int out_size, void* d_ws, size_t ws_size, hipStream_t stream) {
    const u16* x = (const u16*)d_in[0]; const u16* kc = (const u16*)d_in[1]; const u16* vc = (const u16*)d_in[2];
    const u16* wq = (const u16*)d_in[3]; const u16* wo = (const u16*)d_in[4];
    const u16* wr = (const u16*)d_in[6]; const u16* wg = (const u16*)d_in[7]; const u16* wu = (const u16*)d_in[8]; const u16* wd = (const u16*)d_in[9]; const u16* gw = (const u16*)d_in[10];
    float* ws = (float*)d_ws; u16* out = (u16*)d_out;
    k1_proj<<<64 + 56, 256, 0, stream>>>(x, wq, wg, wu, ws);
    k2_attn<<<256, 256, 0, stream>>>(kc, vc, ws);
    k3_out<<<256, 256, 0, stream>>>(x, wo, wd, wr, gw, ws);
    k4_fin<<<16, 256, 0, stream>>>(x, gw, ws, out);
}
```

```cpp
#include <hip/hip_runtime.h>
#include <hip/hip_bf16.h>
#include <cstdint>

typedef unsigned short u16;
typedef unsigned int u32;
typedef unsigned long long u64;
typedef u32 v4u __attribute__((ext_vector_type(4)));
typedef u32 v2u __attribute__((ext_vector_type(2)));
typedef float f32x4 __attribute__((ext_vector_type(4)));
typedef __bf16 bf16x8 __attribute__((ext_vector_type(8)));
#define GAS __attribute__((address_space(1)))
#define LAS __attribute__((address_space(3)))

namespace {
constexpr int D = 1024, HQ = 32, HKV = 4, SK = 65536, DH = 128, FF = 3584;
constexpr int NWG = 256, NTHR = 512, NWAVE = 8;
constexpr int NT_WG = 32;
#define PACE_N 2

constexpr u32 WS_EPOCH = 0;
constexpr u32 WS_QG = 4096;
constexpr u32 WS_QP = WS_QG + 4 * 256 * 16;
constexpr u32 WS_GP = WS_QP + 256 * 64 * 16;
constexpr u32 WS_UP = WS_GP + 256 * 64 * 16;
constexpr u32 WS_ML = WS_UP + 256 * 64 * 16;
constexpr u32 WS_P = WS_ML + 256 * 8 * 16;
constexpr u32 WS_OUTP = WS_P + 256 * 8 * 64 * 16;
constexpr u32 WS_END = WS_OUTP + 256 * 512 * 16;
constexpr int AUX_SC1 = 16;
constexpr u32 SPIN_CAP = 1u << 18;

#define STAMP(k) do { } while (0)
#define STAMPW(k, wv_) do { } while (0)

__device__ __forceinline__ float bflo(u32 w) { return __builtin_bit_cast(float, w << 16); }
__device__ __forceinline__ float bfhi(u32 w) { return __builtin_bit_cast(float, w & 0xffff0000u); }
__device__ __forceinline__ float bf2f(u32 b) { return __builtin_bit_cast(float, b << 16); }
__device__ __forceinline__ u32 cvt1(float v) { return (u32)__builtin_bit_cast(u16, (__bf16)v); }
__device__ __forceinline__ float rbf(float f) { return bf2f(cvt1(f)); }
template <int CTRL> __device__ __forceinline__ float dpp(float x) {
    return __builtin_bit_cast(float, __builtin_amdgcn_mov_dpp(__builtin_bit_cast(int, x), CTRL, 0xf, 0xf, true));
}
template <int CTRL> __device__ __forceinline__ u32 dppu(u32 x) { return (u32)__builtin_amdgcn_mov_dpp((int)x, CTRL, 0xf, 0xf, true); }
constexpr int ROR1 = 0x121, ROR2 = 0x122, ROR4 = 0x124, ROR8 = 0x128, QXOR1 = 0xB1;
__device__ __forceinline__ float xrow_sum(float x) {
    auto s = __builtin_amdgcn_permlane16_swap(__float_as_uint(x), __float_as_uint(x), false, false);
    x = __uint_as_float(s[0]) + __uint_as_float(s[1]);
    auto t = __builtin_amdgcn_permlane32_swap(__float_as_uint(x), __float_as_uint(x), false, false);
    return __uint_as_float(t[0]) + __uint_as_float(t[1]);
}
__device__ __forceinline__ float xrow_max(float x) {
    auto s = __builtin_amdgcn_permlane16_swap(__float_as_uint(x), __float_as_uint(x), false, false);
    x = fmaxf(__uint_as_float(s[0]), __uint_as_float(s[1]));
    auto t = __builtin_amdgcn_permlane32_swap(__float_as_uint(x), __float_as_uint(x), false, false);
    return fmaxf(__uint_as_float(t[0]), __uint_as_float(t[1]));
}
__device__ __forceinline__ f32x4 mfma(v4u a, v4u b, f32x4 c) {
    return __builtin_amdgcn_mfma_f32_16x16x32_bf16(__builtin_bit_cast(bf16x8, a), __builtin_bit_cast(bf16x8, b), c, 0, 0, 0);
}
__device__ __forceinline__ v4u ldnt(const void* p) { return __builtin_nontemporal_load((const GAS v4u*)p); }
__device__ __forceinline__ float ex2(float x) { return __builtin_amdgcn_exp2f(x); }
__device__ __forceinline__ float sigmoidf_(float z) { return 1.f / (1.f + __expf(-z)); }
#define SB() __builtin_amdgcn_sched_barrier(0)
__device__ __forceinline__ v4u poll16(const unsigned char* p) {
    v4u r;
    asm volatile("global_load_dwordx4 %0, %1, off sc1\n\ts_waitcnt vmcnt(0)" : "=&v"(r) : "v"(p) : "memory");
    return r;
}
__device__ __forceinline__ void poll16x2(const unsigned char* p, const unsigned char* q, v4u& a, v4u& b) {
    asm volatile("global_load_dwordx4 %0, %2, off sc1\n\tglobal_load_dwordx4 %1, %3, off sc1\n\ts_waitcnt vmcnt(0)" : "=&v"(a), "=&v"(b) : "v"(p), "v"(q) : "memory");
}
__device__ __forceinline__ bool tagok(const v4u& u, u32 tag) { return u[1] == tag && u[3] == tag; }

struct KV { v4u r[8]; };
__device__ __forceinline__ void issue8(KV& b, const unsigned char* base, u32 voff, const int (&offs)[8]) {
#pragma unroll
    for (int i = 0; i < 8; ++i) b.r[i] = ldnt(base + voff + offs[i]);
    SB();
}
__device__ __forceinline__ void gemv_block(const v4u* wv, const float (&xv)[4], float (&a)[8]) {
#pragma unroll
    for (int k = 0; k < 8; ++k) a[k] = 0.f;
#pragma unroll
    for (int i = 0; i < 4; ++i)
#pragma unroll
        for (int k = 0; k < 4; ++k) { a[2 * k] = fmaf(xv[i], bflo(wv[i][k]), a[2 * k]); a[2 * k + 1] = fmaf(xv[i], bfhi(wv[i][k]), a[2 * k + 1]); }
#pragma unroll
    for (int k = 0; k < 8; ++k) a[k] = xrow_sum(a[k]);
}
}

__global__ void __launch_bounds__(NTHR, 2)
fused_decode(const u16* __restrict__ x, const u16* __restrict__ kc, const u16* __restrict__ vc, const u16* __restrict__ wq, const u16* __restrict__ wo,
             const u16* __restrict__ wr, const u16* __restrict__ wg, const u16* __restrict__ wu, const u16* __restrict__ wd, const u16* __restrict__ gw,
             u16* __restrict__ out, unsigned char* ws)
{
    __shared__ __attribute__((aligned(16))) float lds[8192 + 3 * 1024 + 512 + 512 + 1024];
    float* red = lds;
    float* gpart = lds + 8192;
    float* upart = lds + 8192 + 1024;
    float* qpart = lds + 8192 + 2048;
    float* sm = lds + 8192 + 3072;
    u32* qs = (u32*)(lds + 8192 + 3072 + 512);
    float* xs = lds + 8192 + 3072 + 1024;
    float* gsc = sm;
    float* wml = sm + 32;
    float* cw = sm + 160;
    float* cml = sm + 288;
    float* fin = sm + 320;
    volatile LAS u32* qflag = (volatile LAS u32*)(sm + 384);
    volatile LAS u32* gucnt = (volatile LAS u32*)(sm + 388);

    const int tid = threadIdx.x, lane = tid & 63, w = __builtin_amdgcn_readfirstlane(tid >> 6);
    const int b = blockIdx.x, kvh = b >> 6, rr = b & 63, G = b >> 3, ks = b & 7;
    const __amdgpu_buffer_rsrc_t rs = __builtin_amdgcn_make_buffer_rsrc((void*)ws, 0, (int)WS_END, 0x00020000);
    const bool has_ffn = G < 28;
    const int lc = lane & 15, lr = lane >> 4;
    STAMP(0);

    const u32 E = __hip_atomic_load((GAS u32*)(ws + WS_EPOCH), __ATOMIC_RELAXED, __HIP_MEMORY_SCOPE_AGENT);
    const int grow = 128 * ks + 16 * w + lr;
    u32 xr[4];
#pragma unroll
    for (int i = 0; i < 4; ++i) xr[i] = x[grow + 4 * i];
    v4u wv[4];
#pragma unroll
    for (int i = 0; i < 4; ++i) wv[i] = ldnt(wq + (size_t)(grow + 4 * i) * 4096 + 128 * G + 8 * lc);
    const u32 xp = ((const u32*)x)[tid], gp = ((const u32*)gw)[tid], rp = ((const u32*)wr)[tid];
    if (tid == 0) { *qflag = 0u; *gucnt = 0u; }
    SB();
    const int t0 = NT_WG * rr, cnt = NT_WG / 8;
    const __amdgpu_buffer_rsrc_t rk = __builtin_amdgcn_make_buffer_rsrc((void*)(kc + (size_t)kvh * SK * DH), 0, SK * DH * 2, 0x00020000);
    const __amdgpu_buffer_rsrc_t rv = __builtin_amdgcn_make_buffer_rsrc((void*)(vc + (size_t)kvh * SK * DH), 0, SK * DH * 2, 0x00020000);
    const __amdgpu_buffer_rsrc_t rg = __builtin_amdgcn_make_buffer_rsrc((void*)wg, 0, D * FF * 2, 0x00020000);
    const __amdgpu_buffer_rsrc_t ru = __builtin_amdgcn_make_buffer_rsrc((void*)wu, 0, D * FF * 2, 0x00020000);
    const u32 kvo = (u32)((4 * ((lc >> 1) & 1) + (lc >> 2)) * 256 + (lc & 1) * 64 + lr * 16);
    const u32 vvo = (u32)(lane * 16);
    auto toff = [&](int n) -> u32 { return n < cnt ? (u32)(t0 + w + 8 * n) * 8192u : 0x40000000u; };
#define LD4(R0, R1, R2, R3, VO, RS, O1, O2, O3) asm volatile( \
        "buffer_load_dwordx4 %0, %4, %5, 0 offen nt\n\tbuffer_load_dwordx4 %1, %4, %5, 0 offen offset:" #O1 " nt\n\t" \
        "buffer_load_dwordx4 %2, %4, %5, 0 offen offset:" #O2 " nt\n\tbuffer_load_dwordx4 %3, %4, %5, 0 offen offset:" #O3 " nt" \
        : "=&v"(R0), "=&v"(R1), "=&v"(R2), "=&v"(R3) : "v"(VO), "s"(RS))
#define LD4S(R0, R1, R2, R3, VO, RS, S1, S2, S3) asm volatile( \
        "buffer_load_dwordx4 %0, %4, %5, 0 offen nt\n\tbuffer_load_dwordx4 %1, %4, %5, %6 offen nt\n\t" \
        "buffer_load_dwordx4 %2, %4, %5, %7 offen nt\n\tbuffer_load_dwordx4 %3, %4, %5, %8 offen nt" \
        : "=&v"(R0), "=&v"(R1), "=&v"(R2), "=&v"(R3) : "v"(VO), "s"(RS), "s"(S1), "s"(S2), "s"(S3))
#define LDK4(BUF, H, TO) LD4(BUF.r[4 * (H)], BUF.r[4 * (H) + 1], BUF.r[4 * (H) + 2], BUF.r[4 * (H) + 3], kvo + (TO) + 4096u * (H), rk, 128, 2048, 2176)
#define LDV4(BUF, H, TO) LD4(BUF.r[4 * (H)], BUF.r[4 * (H) + 1], BUF.r[4 * (H) + 2], BUF.r[4 * (H) + 3], vvo + (TO) + 4096u * (H), rv, 1024, 2048, 3072)
#define ISSUE_K(BUF, TO) do { LDK4(BUF, 0, TO); LDK4(BUF, 1, TO); } while (0)
#define ISSUE_V(BUF, TO) do { LDV4(BUF, 0, TO); LDV4(BUF, 1, TO); } while (0)
#define WAIT8(BUF, N) asm volatile("s_waitcnt vmcnt(" #N ")" : "+v"(BUF.r[0]), "+v"(BUF.r[1]), "+v"(BUF.r[2]), "+v"(BUF.r[3]), "+v"(BUF.r[4]), "+v"(BUF.r[5]), "+v"(BUF.r[6]), "+v"(BUF.r[7]))
#define STR2(x) #x
#define STR(x) STR2(x)
#define PACE() do { SB(); if (__builtin_amdgcn_readfirstlane(*qflag) == 0u) asm volatile("s_waitcnt vmcnt(" STR(PACE_N) ")" ::: "memory"); SB(); } while (0)
    const u32 to0 = toff(0), to1 = toff(1);
    KV KA, VA, KB, VB;

    float xv[4];
#pragma unroll
    for (int i = 0; i < 4; ++i) xv[i] = bf2f(xr[i]);
    {
        float a[8];
        gemv_block(wv, xv, a);
        if (lr == 0) { float* dst = qpart + w * 128 + 8 * lc; *(f32x4*)dst = f32x4{a[0], a[1], a[2], a[3]}; *(f32x4*)(dst + 4) = f32x4{a[4], a[5], a[6], a[7]}; }
        float z = rbf(bflo(xp) * bflo(gp)) + rbf(bfhi(xp) * bfhi(gp));
        float r = bflo(xp) * bflo(rp) + bfhi(xp) * bfhi(rp);
        z += dpp<ROR1>(z); z += dpp<ROR2>(z); z += dpp<ROR4>(z); z += dpp<ROR8>(z); z = xrow_sum(z);
        r += dpp<ROR1>(r); r += dpp<ROR2>(r); r += dpp<ROR4>(r); r += dpp<ROR8>(r); r = xrow_sum(r);
        if (lane == 0) { gsc[2 * w] = z; gsc[2 * w + 1] = r; }
    }
    const u32 guo = has_ffn ? (u32)((grow * FF + 128 * G + 8 * lc) * 2) : 0x40000000u;
    if (w != 0) LD4S(KB.r[0], KB.r[1], KB.r[2], KB.r[3], guo, rg, 4 * FF * 2, 8 * FF * 2, 12 * FF * 2);
    SB();
    __syncthreads();
    STAMP(1);
    const u32 tag = __builtin_amdgcn_readfirstlane(E) + 1u;
    if (w == 0) {
        float s0 = 0.f, s1 = 0.f;
#pragma unroll
        for (int ww = 0; ww < 8; ++ww) { const float2 v = *(const float2*)(qpart + ww * 128 + 2 * lane); s0 += v.x; s1 += v.y; }
        v4u u = {__float_as_uint(s0), tag, __float_as_uint(s1), tag};
        __builtin_amdgcn_raw_buffer_store_b128(u, rs, (int)(WS_QP + (b * 64 + lane) * 16), 0, AUX_SC1);
        const unsigned char* qpa = ws + WS_QP + ((G * 8 + (lane >> 3)) * 64 + ks * 8 + (lane & 7)) * 16;
        v4u qa;
        u32 spins = 0;
        for (;;) {
            qa = poll16(qpa);
            if (__all(tagok(qa, tag)) || ++spins > SPIN_CAP) break;
            __builtin_amdgcn_s_sleep(1);
        }
        STAMP(11);
        s0 = __uint_as_float(qa[0]); s1 = __uint_as_float(qa[2]);
        s0 += dpp<ROR8>(s0); s1 += dpp<ROR8>(s1); s0 = xrow_sum(s0); s1 = xrow_sum(s1);
        const u32 pr = cvt1(s0) | (cvt1(s1) << 16);
        const u32 nb = dppu<QXOR1>(pr);
        if (lane < 8 && (lane & 1) == 0) {
            v4u uq = {pr, tag, nb, tag};
            __builtin_amdgcn_raw_buffer_store_b128(uq, rs, (int)(WS_QG + (kvh * 256 + rr * 4 + (lane >> 1)) * 16), 0, AUX_SC1);
        }
        const unsigned char* qp = ws + WS_QG + (kvh * 256 + lane) * 16;
        v4u q0, q1u, q2u, q3u;
        spins = 0;
        for (;;) {
            asm volatile("global_load_dwordx4 %0, %4, off sc1\n\tglobal_load_dwordx4 %1, %4, off offset:1024 sc1\n\tglobal_load_dwordx4 %2, %4, off offset:2048 sc1\n\tglobal_load_dwordx4 %3, %4, off offset:3072 sc1\n\ts_waitcnt vmcnt(0)"
                         : "=&v"(q0), "=&v"(q1u), "=&v"(q2u), "=&v"(q3u) : "v"(qp) : "memory");
            const bool ok = tagok(q0, tag) && tagok(q1u, tag) && tagok(q2u, tag) && tagok(q3u, tag);
            if (__all(ok) || ++spins > SPIN_CAP) break;
            __builtin_amdgcn_s_sleep(1);
        }
        *(v2u*)(qs + lane * 2) = v2u{q0[0], q0[2]};
        *(v2u*)(qs + (64 + lane) * 2) = v2u{q1u[0], q1u[2]};
        *(v2u*)(qs + (128 + lane) * 2) = v2u{q2u[0], q2u[2]};
        *(v2u*)(qs + (192 + lane) * 2) = v2u{q3u[0], q3u[2]};
        asm volatile("s_waitcnt lgkmcnt(0)" ::: "memory");
        *qflag = 1u;
        STAMP(12);
        LD4S(KB.r[0], KB.r[1], KB.r[2], KB.r[3], guo, rg, 4 * FF * 2, 8 * FF * 2, 12 * FF * 2);
    }
    PACE();
    LD4S(KB.r[4], KB.r[5], KB.r[6], KB.r[7], guo, ru, 4 * FF * 2, 8 * FF * 2, 12 * FF * 2); PACE();
    LDK4(KA, 0, to0);
    WAIT8(KB, 4);
    if (has_ffn) {
        float ag[8], au[8];
        gemv_block(&KB.r[0], xv, ag);
        gemv_block(&KB.r[4], xv, au);
        if (lr == 0) {
            float* dg = gpart + w * 128 + 8 * lc; float* du = upart + w * 128 + 8 * lc;
            *(f32x4*)dg = f32x4{ag[0], ag[1], ag[2], ag[3]}; *(f32x4*)(dg + 4) = f32x4{ag[4], ag[5], ag[6], ag[7]};
            *(f32x4*)du = f32x4{au[0], au[1], au[2], au[3]}; *(f32x4*)(du + 4) = f32x4{au[4], au[5], au[6], au[7]};
        }
        asm volatile("s_waitcnt lgkmcnt(0)" ::: "memory");
        u32 old = 0;
        if (lane == 0) old = __hip_atomic_fetch_add((LAS u32*)gucnt, 1u, __ATOMIC_RELAXED, __HIP_MEMORY_SCOPE_WORKGROUP);
        if (__builtin_amdgcn_readfirstlane(old) == 7u) {
            float g0 = 0.f, g1 = 0.f, u0 = 0.f, u1 = 0.f;
#pragma unroll
            for (int ww = 0; ww < 8; ++ww) { const float2 vg = *(const float2*)(gpart + ww * 128 + 2 * lane), vu = *(const float2*)(upart + ww * 128 + 2 * lane); g0 += vg.x; g1 += vg.y; u0 += vu.x; u1 += vu.y; }
            v4u ug = {__float_as_uint(g0), tag, __float_as_uint(g1), tag}, uu_ = {__float_as_uint(u0), tag, __float_as_uint(u1), tag};
            __builtin_amdgcn_raw_buffer_store_b128(ug, rs, (int)(WS_GP + (b * 64 + lane) * 16), 0, AUX_SC1);
            __builtin_amdgcn_raw_buffer_store_b128(uu_, rs, (int)(WS_UP + (b * 64 + lane) * 16), 0, AUX_SC1);
        }
    }
    STAMPW(2, 1);
    PACE();
    LDK4(KA, 1, to0); PACE();
    LDV4(VA, 0, to0); PACE();
    LDV4(VA, 1, to0); PACE();
    LDK4(KB, 0, to1); PACE();
    LDK4(KB, 1, to1); PACE();
    LDV4(VB, 0, to1); PACE();
    LDV4(VB, 1, to1);
    {
        u32 spins = 0;
        while (*qflag == 0u) { __builtin_amdgcn_s_sleep(1); if (++spins > 64u * SPIN_CAP) break; }
        STAMPW(13, 1);
    }
    v4u qf[2];
#pragma unroll
    for (int s = 0; s < 2; ++s) qf[s] = *(const v4u*)(qs + (lane & 7) * 64 + 32 * s + 16 * ((lane >> 3) & 1) + 4 * lr);

    constexpr float C2 = 0.08838834764831845f * 1.4426950408889634f;
    const bool uhi = (lane >> 3) & 1;
    const u32 psh = uhi ? 16u : 0u;
    float m_run = -INFINITY, l_run = 0.f;
    f32x4 acc[8];
#pragma unroll
    for (int d = 0; d < 8; ++d) acc[d] = f32x4{0.f, 0.f, 0.f, 0.f};

    auto qk = [&](const KV& Kb, float (&sc)[4]) {
#pragma unroll
        for (int t = 0; t < 4; ++t) {
            f32x4 c = {0.f, 0.f, 0.f, 0.f};
            c = mfma(Kb.r[2 * t], qf[0], c);
            c = mfma(Kb.r[2 * t + 1], qf[1], c);
            const float t0 = c[0] + dpp<ROR8>(c[1]);
            const float t1 = c[3] + dpp<ROR8>(c[2]);
            sc[t] = uhi ? t1 : t0;
        }
    };
    auto softmax_pv = [&](const float (&sc)[4], const KV& Vb) {
        float mx = fmaxf(fmaxf(sc[0], sc[1]), fmaxf(sc[2], sc[3]));
        mx = fmaxf(mx, dpp<ROR8>(mx));
        mx = xrow_max(mx);
        const float mn = fmaxf(m_run, mx * C2);
        const float alpha = ex2(m_run - mn);
        m_run = mn;
        v4u pf;
        float ps = 0.f;
#pragma unroll
        for (int t = 0; t < 4; ++t) { const float p = ex2(fmaf(sc[t], C2, -mn)); const u32 pb = cvt1(p); ps += bf2f(pb); pf[t] = pb << psh; }
        l_run = fmaf(l_run, alpha, ps);
#pragma unroll
        for (int d = 0; d < 8; ++d) acc[d] *= alpha;
#pragma unroll
        for (int d = 0; d < 8; ++d) {
            v4u a;
#pragma unroll
            for (int q = 0; q < 4; ++q) a[q] = __builtin_amdgcn_perm(Vb.r[2 * q + 1][d >> 1], Vb.r[2 * q][d >> 1], (d & 1) ? 0x07060302u : 0x05040100u);
            acc[d] = mfma(a, pf, acc[d]);
        }
    };
    const int ocol = 128 * w + 8 * lc;
    float sc[4];
    for (int n = 0; n < cnt; n += 2) {
        const u32 ta = toff(n + 2), tb = toff(n + 3);
        WAIT8(KA, 24);
        qk(KA, sc); SB();
        ISSUE_K(KA, ta);
        WAIT8(VA, 24);
        softmax_pv(sc, VA); SB();
        ISSUE_V(VA, ta);
        if (n == 0) STAMP(3);
        const bool vb = n + 1 < cnt;
        WAIT8(KB, 24);
        if (vb) qk(KB, sc);
        SB();
        ISSUE_K(KB, tb);
        WAIT8(VB, 24);
        if (vb) softmax_pv(sc, VB);
        SB();
        ISSUE_V(VB, tb);
        if (n == 0) STAMP(4);
    }
    WAIT8(KA, 0); WAIT8(VA, 0); WAIT8(KB, 0); WAIT8(VB, 0);
    const unsigned char* gpa = ws + WS_GP + ((G * 8 + (lane >> 3)) * 64 + ks * 8 + (lane & 7)) * 16;
    const unsigned char* upa = ws + WS_UP + ((G * 8 + (lane >> 3)) * 64 + ks * 8 + (lane & 7)) * 16;
    v4u gu = {0, 0, 0, 0}, uu = {0, 0, 0, 0};
    if (has_ffn) {
        gu = __builtin_amdgcn_raw_buffer_load_b128(rs, (int)(gpa - ws), 0, AUX_SC1);
        uu = __builtin_amdgcn_raw_buffer_load_b128(rs, (int)(upa - ws), 0, AUX_SC1);
#pragma unroll
        for (int i = 0; i < 4; ++i) VA.r[i] = ldnt(wd + (size_t)(16 * b + 4 * i + lr) * D + ocol);
    } else {
#pragma unroll
        for (int i = 0; i < 4; ++i) VA.r[i] = v4u{0u, 0u, 0u, 0u};
    }
#pragma unroll
    for (int i = 0; i < 4; ++i) VA.r[4 + i] = ldnt(wo + (size_t)(16 * b + 4 * i + lr) * D + ocol);
    SB();
    STAMP(5);
    STAMPW(14, 7);

    {
        l_run += dpp<ROR8>(l_run); l_run = xrow_sum(l_run);
#pragma unroll
        for (int d = 0; d < 8; ++d)
#pragma unroll
            for (int i = 0; i < 4; ++i) acc[d][i] += dpp<ROR8>(acc[d][i]);
        if (!uhi) {
            float* dst = red + (w * 8 + (lane & 7)) * 128 + 32 * lr;
#pragma unroll
            for (int i = 0; i < 4; ++i) {
                *(f32x4*)(dst + 8 * i) = f32x4{acc[0][i], acc[1][i], acc[2][i], acc[3][i]};
                *(f32x4*)(dst + 8 * i + 4) = f32x4{acc[4][i], acc[5][i], acc[6][i], acc[7][i]};
            }
            if (lr == 0) { wml[(w * 8 + (lane & 7)) * 2] = m_run; wml[(w * 8 + (lane & 7)) * 2 + 1] = l_run; }
        }
    }
    __syncthreads();
    {
        float M = -INFINITY;
#pragma unroll
        for (int ww = 0; ww < 8; ++ww) M = fmaxf(M, wml[(ww * 8 + w) * 2]);
        float L = 0.f, o0 = 0.f, o1 = 0.f;
#pragma unroll
        for (int ww = 0; ww < 8; ++ww) {
            const float f = ex2(wml[(ww * 8 + w) * 2] - M);
            L = fmaf(f, wml[(ww * 8 + w) * 2 + 1], L);
            const float2 v = *(const float2*)(red + (ww * 8 + w) * 128 + 2 * lane);
            o0 = fmaf(f, v.x, o0); o1 = fmaf(f, v.y, o1);
        }
        v4u u = {__float_as_uint(o0), tag, __float_as_uint(o1), tag};
        __builtin_amdgcn_raw_buffer_store_b128(u, rs, (int)(WS_P + ((b * 8 + w) * 64 + lane) * 16), 0, AUX_SC1);
        if (lane == 0) { v4u ml = {__float_as_uint(M), tag, __float_as_uint(L), tag};
            __builtin_amdgcn_raw_buffer_store_b128(ml, rs, (int)(WS_ML + (b * 8 + w) * 16), 0, AUX_SC1); }
    }
    STAMP(6);
    const int hq_l = rr >> 3, d0p = (rr & 7) * 8;
    const int src = kvh * 64 + (tid >> 3);
    const int poff = (int)(WS_P + ((src * 8 + hq_l) * 64 + d0p + (tid & 7)) * 16), mloff = (int)(WS_ML + (src * 8 + hq_l) * 16);
    v4u pv = __builtin_amdgcn_raw_buffer_load_b128(rs, poff, 0, AUX_SC1);
    v4u ml = __builtin_amdgcn_raw_buffer_load_b128(rs, mloff, 0, AUX_SC1);
    SB();
    float z = 0.f, r = 0.f;
#pragma unroll
    for (int ww = 0; ww < 8; ++ww) { z += gsc[2 * ww]; r += gsc[2 * ww + 1]; }
    const float g = rbf(sigmoidf_(rbf(z))), sr = rbf(sigmoidf_(rbf(r)));
    const float omg = rbf(1.f - g);
    float h0 = 0.f, h1 = 0.f;
    if (has_ffn) {
        u32 spins = 0;
        while (!__all(tagok(gu, tag) && tagok(uu, tag))) {
            __builtin_amdgcn_s_sleep(1);
            poll16x2(gpa, upa, gu, uu);
            if (++spins > SPIN_CAP) break;
        }
        float g0 = __uint_as_float(gu[0]), g1 = __uint_as_float(gu[2]), u0 = __uint_as_float(uu[0]), u1 = __uint_as_float(uu[2]);
        g0 += dpp<ROR8>(g0); g1 += dpp<ROR8>(g1); u0 += dpp<ROR8>(u0); u1 += dpp<ROR8>(u1);
        g0 = rbf(xrow_sum(g0)); g1 = rbf(xrow_sum(g1)); u0 = rbf(xrow_sum(u0)); u1 = rbf(xrow_sum(u1));
        h0 = rbf(rbf(g0 * sigmoidf_(g0)) * u0) * (omg * sr);
        h1 = rbf(rbf(g1 * sigmoidf_(g1)) * u1) * (omg * sr);
    }
    float a[8];
#pragma unroll
    for (int k = 0; k < 8; ++k) a[k] = 0.f;
#pragma unroll
    for (int i = 0; i < 4; ++i) {
        const int row = 4 * i + lr;
        const float c0 = __shfl(h0, row >> 1), c1 = __shfl(h1, row >> 1);
        const float ch = (row & 1) ? c1 : c0;
#pragma unroll
        for (int k = 0; k < 4; ++k) { a[2 * k] = fmaf(ch, bflo(VA.r[i][k]), a[2 * k]); a[2 * k + 1] = fmaf(ch, bfhi(VA.r[i][k]), a[2 * k + 1]); }
    }
    STAMP(7);
    {
        u32 spins = 0;
        while (!__all(tagok(pv, tag) && tagok(ml, tag))) {
            __builtin_amdgcn_s_sleep(1);
            poll16x2(ws + poff, ws + mloff, pv, ml);
            if (++spins > SPIN_CAP) break;
        }
        const float ms = __uint_as_float(ml[0]), ls = __uint_as_float(ml[2]);
        float Mw = fmaxf(ms, dpp<ROR8>(ms)); Mw = xrow_max(Mw);
        const float e = ex2(ms - Mw);
        float L = e * ls, o0 = e * __uint_as_float(pv[0]), o1 = e * __uint_as_float(pv[2]);
        L += dpp<ROR8>(L); o0 += dpp<ROR8>(o0); o1 += dpp<ROR8>(o1);
        L = xrow_sum(L); o0 = xrow_sum(o0); o1 = xrow_sum(o1);
        if (lane < 8) *(float2*)(cw + w * 16 + 2 * lane) = make_float2(o0, o1);
        if (lane == 0) { cml[2 * w] = Mw; cml[2 * w + 1] = L; }
    }
    __syncthreads();
    STAMP(8);
    {
        float M = -INFINITY;
#pragma unroll
        for (int ww = 0; ww < 8; ++ww) M = fmaxf(M, cml[2 * ww]);
        float L = 0.f, o = 0.f;
#pragma unroll
        for (int ww = 0; ww < 8; ++ww) { const float f = ex2(cml[2 * ww] - M); L = fmaf(f, cml[2 * ww + 1], L); o = fmaf(f, cw[ww * 16 + lc], o); }
        const float ov = rbf(o / L) * g;
#pragma unroll
        for (int i = 0; i < 4; ++i) {
            const float co = __shfl(ov, 4 * i + lr);
#pragma unroll
            for (int k = 0; k < 4; ++k) { a[2 * k] = fmaf(co, bflo(VA.r[4 + i][k]), a[2 * k]); a[2 * k + 1] = fmaf(co, bfhi(VA.r[4 + i][k]), a[2 * k + 1]); }
        }
#pragma unroll
        for (int k = 0; k < 8; ++k) a[k] = xrow_sum(a[k]);
        const float v0 = lr == 0 ? a[0] : lr == 1 ? a[2] : lr == 2 ? a[4] : a[6];
        const float v1 = lr == 0 ? a[1] : lr == 1 ? a[3] : lr == 2 ? a[5] : a[7];
        v4u u = {__float_as_uint(v0), tag, __float_as_uint(v1), tag};
        __builtin_amdgcn_raw_buffer_store_b128(u, rs, (int)(WS_OUTP + (b * 512 + (ocol >> 1) + lr) * 16), 0, AUX_SC1);
        STAMP(9);
        const unsigned char* fp = ws + WS_OUTP + ((tid >> 1) * 512 + 2 * b + (tid & 1)) * 16;
        v4u fv;
        u32 spins = 0;
        for (;;) {
            fv = poll16(fp);
            if (__all(tagok(fv, tag)) || ++spins > SPIN_CAP) break;
            __builtin_amdgcn_s_sleep(1);
        }
        STAMP(10);
        float f0 = __uint_as_float(fv[0]), f1 = __uint_as_float(fv[2]);
        f0 += dpp<ROR2>(f0); f0 += dpp<ROR4>(f0); f0 += dpp<ROR8>(f0); f0 = xrow_sum(f0);
        f1 += dpp<ROR2>(f1); f1 += dpp<ROR4>(f1); f1 += dpp<ROR8>(f1); f1 = xrow_sum(f1);
        if (lane < 2) { fin[w * 4 + 2 * lane] = f0; fin[w * 4 + 2 * lane + 1] = f1; }
        __syncthreads();
        if (tid < 4) {
            float s = 0.f;
#pragma unroll
            for (int ww = 0; ww < 8; ++ww) s += fin[ww * 4 + tid];
            const float xo = bf2f(x[4 * b + tid]);
            out[4 * b + tid] = (u16)cvt1(s + omg * xo);
        }
        if (b == 0 && tid == 0) __hip_atomic_store((GAS u32*)(ws + WS_EPOCH), tag, __ATOMIC_RELAXED, __HIP_MEMORY_SCOPE_AGENT);
    }
    STAMP(15);
}

extern "C" void kernel_launch(void* const* d_in, const int* in_sizes, int n_in, void* d_out, int out_size, void* d_ws, size_t ws_size, hipStream_t stream) {
    const u16* x = (const u16*)d_in[0]; const u16* kc = (const u16*)d_in[1]; const u16* vc = (const u16*)d_in[2];
    const u16* wq = (const u16*)d_in[3]; const u16* wo = (const u16*)d_in[4];
    const u16* wr = (const u16*)d_in[6]; const u16* wg = (const u16*)d_in[7]; const u16* wu = (const u16*)d_in[8]; const u16* wd = (const u16*)d_in[9]; const u16* gw = (const u16*)d_in[10];
    fused_decode<<<NWG, NTHR, 0, stream>>>(x, kc, vc, wq, wo, wr, wg, wu, wd, gw, (u16*)d_out, (unsigned char*)d_ws);
}
```

```cpp
#include <hip/hip_runtime.h>
#include <hip/hip_bf16.h>
#include <cstdint>
#include <cstdio>
#define STAMPS 1
#define PRINT_STAMPS 1

typedef unsigned short u16;
typedef unsigned int u32;
typedef unsigned long long u64;
typedef u32 v4u __attribute__((ext_vector_type(4)));
typedef u32 v2u __attribute__((ext_vector_type(2)));
typedef float f32x4 __attribute__((ext_vector_type(4)));
typedef __bf16 bf16x8 __attribute__((ext_vector_type(8)));
#define GAS __attribute__((address_space(1)))
#define LAS __attribute__((address_space(3)))

namespace {
constexpr int D = 1024, HQ = 32, HKV = 4, SK = 65536, DH = 128, FF = 3584;
constexpr int NWG = 256, NTHR = 512, NWAVE = 8;
constexpr int NT_WG = 32;
#define PACE_N 2

constexpr u32 WS_EPOCH = 0;
constexpr u32 WS_QG = 4096;
constexpr u32 WS_QP = WS_QG + 4 * 256 * 16;
constexpr u32 WS_GP = WS_QP + 256 * 64 * 16;
constexpr u32 WS_UP = WS_GP + 256 * 64 * 16;
constexpr u32 WS_ML = WS_UP + 256 * 64 * 16;
constexpr u32 WS_P = WS_ML + 256 * 8 * 16;
constexpr u32 WS_OUTP = WS_P + 256 * 8 * 64 * 16;
constexpr u32 WS_END = WS_OUTP + 256 * 512 * 16;
constexpr int AUX_SC1 = 16;
constexpr u32 SPIN_CAP = 1u << 18;

constexpr int NSTAMP = 16;
constexpr size_t STAMP_OFF = 16u << 20;
constexpr int XCD_STAMP = 14;
[[maybe_unused]] static const char* STAMP_NAMES[NSTAMP] = {"start", "barA (q part)", "gate/up done", "tile0 done", "tile1 done", "attn done(w0)", "partial published", "h done", "combine polled", "outp published", "final polled", "w0: pollA ok", "w0: pollB ok", "w1: flag seen", "w7: attn done", "end"};
#define STAMPW(k, wv_) do { if (threadIdx.x == 64 * (wv_)) ((GAS u64*)(ws + STAMP_OFF))[blockIdx.x * NSTAMP + (k)] = __builtin_amdgcn_s_memrealtime(); } while (0)
#define STAMP(k) STAMPW(k, 0)

__device__ __forceinline__ float bflo(u32 w) { return __builtin_bit_cast(float, w << 16); }
__device__ __forceinline__ float bfhi(u32 w) { return __builtin_bit_cast(float, w & 0xffff0000u); }
__device__ __forceinline__ float bf2f(u32 b) { return __builtin_bit_cast(float, b << 16); }
__device__ __forceinline__ u32 cvt1(float v) { return (u32)__builtin_bit_cast(u16, (__bf16)v); }
__device__ __forceinline__ float rbf(float f) { return bf2f(cvt1(f)); }
template <int CTRL> __device__ __forceinline__ float dpp(float x) {
    return __builtin_bit_cast(float, __builtin_amdgcn_mov_dpp(__builtin_bit_cast(int, x), CTRL, 0xf, 0xf, true));
}
template <int CTRL> __device__ __forceinline__ u32 dppu(u32 x) { return (u32)__builtin_amdgcn_mov_dpp((int)x, CTRL, 0xf, 0xf, true); }
constexpr int ROR1 = 0x121, ROR2 = 0x122, ROR4 = 0x124, ROR8 = 0x128, QXOR1 = 0xB1;
__device__ __forceinline__ float xrow_sum(float x) {
    auto s = __builtin_amdgcn_permlane16_swap(__float_as_uint(x), __float_as_uint(x), false, false);
    x = __uint_as_float(s[0]) + __uint_as_float(s[1]);
    auto t = __builtin_amdgcn_permlane32_swap(__float_as_uint(x), __float_as_uint(x), false, false);
    return __uint_as_float(t[0]) + __uint_as_float(t[1]);
}
__device__ __forceinline__ float xrow_max(float x) {
    auto s = __builtin_amdgcn_permlane16_swap(__float_as_uint(x), __float_as_uint(x), false, false);
    x = fmaxf(__uint_as_float(s[0]), __uint_as_float(s[1]));
    auto t = __builtin_amdgcn_permlane32_swap(__float_as_uint(x), __float_as_uint(x), false, false);
    return fmaxf(__uint_as_float(t[0]), __uint_as_float(t[1]));
}
__device__ __forceinline__ f32x4 mfma(v4u a, v4u b, f32x4 c) {
    return __builtin_amdgcn_mfma_f32_16x16x32_bf16(__builtin_bit_cast(bf16x8, a), __builtin_bit_cast(bf16x8, b), c, 0, 0, 0);
}
__device__ __forceinline__ v4u ldnt(const void* p) { return __builtin_nontemporal_load((const GAS v4u*)p); }
__device__ __forceinline__ float ex2(float x) { return __builtin_amdgcn_exp2f(x); }
__device__ __forceinline__ float sigmoidf_(float z) { return 1.f / (1.f + __expf(-z)); }
#define SB() __builtin_amdgcn_sched_barrier(0)
__device__ __forceinline__ v4u poll16(const unsigned char* p) {
    v4u r;
    asm volatile("global_load_dwordx4 %0, %1, off sc1\n\ts_waitcnt vmcnt(0)" : "=&v"(r) : "v"(p) : "memory");
    return r;
}
__device__ __forceinline__ void poll16x2(const unsigned char* p, const unsigned char* q, v4u& a, v4u& b) {
    asm volatile("global_load_dwordx4 %0, %2, off sc1\n\tglobal_load_dwordx4 %1, %3, off sc1\n\ts_waitcnt vmcnt(0)" : "=&v"(a), "=&v"(b) : "v"(p), "v"(q) : "memory");
}
__device__ __forceinline__ bool tagok(const v4u& u, u32 tag) { return u[1] == tag && u[3] == tag; }

struct KV { v4u r[8]; };
__device__ __forceinline__ void issue8(KV& b, const unsigned char* base, u32 voff, const int (&offs)[8]) {
#pragma unroll
    for (int i = 0; i < 8; ++i) b.r[i] = ldnt(base + voff + offs[i]);
    SB();
}
__device__ __forceinline__ void gemv_block(const v4u* wv, const float (&xv)[4], float (&a)[8]) {
#pragma unroll
    for (int k = 0; k < 8; ++k) a[k] = 0.f;
#pragma unroll
    for (int i = 0; i < 4; ++i)
#pragma unroll
        for (int k = 0; k < 4; ++k) { a[2 * k] = fmaf(xv[i], bflo(wv[i][k]), a[2 * k]); a[2 * k + 1] = fmaf(xv[i], bfhi(wv[i][k]), a[2 * k + 1]); }
#pragma unroll
    for (int k = 0; k < 8; ++k) a[k] = xrow_sum(a[k]);
}
}

__global__ void __launch_bounds__(NTHR, 2)
fused_decode(const u16* __restrict__ x, const u16* __restrict__ kc, const u16* __restrict__ vc, const u16* __restrict__ wq, const u16* __restrict__ wo,
             const u16* __restrict__ wr, const u16* __restrict__ wg, const u16* __restrict__ wu, const u16* __restrict__ wd, const u16* __restrict__ gw,
             u16* __restrict__ out, unsigned char* ws)
{
    __shared__ __attribute__((aligned(16))) float lds[8192 + 3 * 1024 + 512 + 512 + 1024];
    float* red = lds;
    float* gpart = lds + 8192;
    float* upart = lds + 8192 + 1024;
    float* qpart = lds + 8192 + 2048;
    float* sm = lds + 8192 + 3072;
    u32* qs = (u32*)(lds + 8192 + 3072 + 512);
    float* xs = lds + 8192 + 3072 + 1024;
    float* gsc = sm;
    float* wml = sm + 32;
    float* cw = sm + 160;
    float* cml = sm + 288;
    float* fin = sm + 320;
    volatile LAS u32* qflag = (volatile LAS u32*)(sm + 384);
    volatile LAS u32* gucnt = (volatile LAS u32*)(sm + 388);

    const int tid = threadIdx.x, lane = tid & 63, w = __builtin_amdgcn_readfirstlane(tid >> 6);
    const int b = blockIdx.x, kvh = b >> 6, rr = b & 63, G = b >> 3, ks = b & 7;
    const __amdgpu_buffer_rsrc_t rs = __builtin_amdgcn_make_buffer_rsrc((void*)ws, 0, (int)WS_END, 0x00020000);
    const bool has_ffn = G < 28;
    const int lc = lane & 15, lr = lane >> 4;
    STAMP(0);

    const u32 E = __hip_atomic_load((GAS u32*)(ws + WS_EPOCH), __ATOMIC_RELAXED, __HIP_MEMORY_SCOPE_AGENT);
    const int grow = 128 * ks + 16 * w + lr;
    u32 xr[4];
#pragma unroll
    for (int i = 0; i < 4; ++i) xr[i] = x[grow + 4 * i];
    v4u wv[4];
#pragma unroll
    for (int i = 0; i < 4; ++i) wv[i] = ldnt(wq + (size_t)(grow + 4 * i) * 4096 + 128 * G + 8 * lc);
    const u32 xp = ((const u32*)x)[tid], gp = ((const u32*)gw)[tid], rp = ((const u32*)wr)[tid];
    if (tid == 0) { *qflag = 0u; *gucnt = 0u; }
    SB();
    const int t0 = NT_WG * rr, cnt = NT_WG / 8;
    const __amdgpu_buffer_rsrc_t rk = __builtin_amdgcn_make_buffer_rsrc((void*)(kc + (size_t)kvh * SK * DH), 0, SK * DH * 2, 0x00020000);
    const __amdgpu_buffer_rsrc_t rv = __builtin_amdgcn_make_buffer_rsrc((void*)(vc + (size_t)kvh * SK * DH), 0, SK * DH * 2, 0x00020000);
    const __amdgpu_buffer_rsrc_t rg = __builtin_amdgcn_make_buffer_rsrc((void*)wg, 0, D * FF * 2, 0x00020000);
    const __amdgpu_buffer_rsrc_t ru = __builtin_amdgcn_make_buffer_rsrc((void*)wu, 0, D * FF * 2, 0x00020000);
    const u32 kvo = (u32)((4 * ((lc >> 1) & 1) + (lc >> 2)) * 256 + (lc & 1) * 64 + lr * 16);
    const u32 vvo = (u32)(lane * 16);
    auto toff = [&](int n) -> u32 { return n < cnt ? (u32)(t0 + w + 8 * n) * 8192u : 0x40000000u; };
#define LD4(R0, R1, R2, R3, VO, RS, O1, O2, O3) asm volatile( \
        "buffer_load_dwordx4 %0, %4, %5, 0 offen nt\n\tbuffer_load_dwordx4 %1, %4, %5, 0 offen offset:" #O1 " nt\n\t" \
        "buffer_load_dwordx4 %2, %4, %5, 0 offen offset:" #O2 " nt\n\tbuffer_load_dwordx4 %3, %4, %5, 0 offen offset:" #O3 " nt" \
        : "=&v"(R0), "=&v"(R1), "=&v"(R2), "=&v"(R3) : "v"(VO), "s"(RS))
#define LD4S(R0, R1, R2, R3, VO, RS, S1, S2, S3) asm volatile( \
        "buffer_load_dwordx4 %0, %4, %5, 0 offen nt\n\tbuffer_load_dwordx4 %1, %4, %5, %6 offen nt\n\t" \
        "buffer_load_dwordx4 %2, %4, %5, %7 offen nt\n\tbuffer_load_dwordx4 %3, %4, %5, %8 offen nt" \
        : "=&v"(R0), "=&v"(R1), "=&v"(R2), "=&v"(R3) : "v"(VO), "s"(RS), "s"(S1), "s"(S2), "s"(S3))
#define LDK4(BUF, H, TO) LD4(BUF.r[4 * (H)], BUF.r[4 * (H) + 1], BUF.r[4 * (H) + 2], BUF.r[4 * (H) + 3], kvo + (TO) + 4096u * (H), rk, 128, 2048, 2176)
#define LDV4(BUF, H, TO) LD4(BUF.r[4 * (H)], BUF.r[4 * (H) + 1], BUF.r[4 * (H) + 2], BUF.r[4 * (H) + 3], vvo + (TO) + 4096u * (H), rv, 1024, 2048, 3072)
#define ISSUE_K(BUF, TO) do { LDK4(BUF, 0, TO); LDK4(BUF, 1, TO); } while (0)
#define ISSUE_V(BUF, TO) do { LDV4(BUF, 0, TO); LDV4(BUF, 1, TO); } while (0)
#define WAIT8(BUF, N) asm volatile("s_waitcnt vmcnt(" #N ")" : "+v"(BUF.r[0]), "+v"(BUF.r[1]), "+v"(BUF.r[2]), "+v"(BUF.r[3]), "+v"(BUF.r[4]), "+v"(BUF.r[5]), "+v"(BUF.r[6]), "+v"(BUF.r[7]))
#define STR2(x) #x
#define STR(x) STR2(x)
#define PACE() do { SB(); if (__builtin_amdgcn_readfirstlane(*qflag) == 0u) asm volatile("s_waitcnt vmcnt(" STR(PACE_N) ")" ::: "memory"); SB(); } while (0)
    const u32 to0 = toff(0), to1 = toff(1);
    KV KA, VA, KB, VB;

    float xv[4];
#pragma unroll
    for (int i = 0; i < 4; ++i) xv[i] = bf2f(xr[i]);
    {
        float a[8];
        gemv_block(wv, xv, a);
        if (lr == 0) { float* dst = qpart + w * 128 + 8 * lc; *(f32x4*)dst = f32x4{a[0], a[1], a[2], a[3]}; *(f32x4*)(dst + 4) = f32x4{a[4], a[5], a[6], a[7]}; }
        float z = rbf(bflo(xp) * bflo(gp)) + rbf(bfhi(xp) * bfhi(gp));
        float r = bflo(xp) * bflo(rp) + bfhi(xp) * bfhi(rp);
        z += dpp<ROR1>(z); z += dpp<ROR2>(z); z += dpp<ROR4>(z); z += dpp<ROR8>(z); z = xrow_sum(z);
        r += dpp<ROR1>(r); r += dpp<ROR2>(r); r += dpp<ROR4>(r); r += dpp<ROR8>(r); r = xrow_sum(r);
        if (lane == 0) { gsc[2 * w] = z; gsc[2 * w + 1] = r; }
    }
    const u32 guo = has_ffn ? (u32)((grow * FF + 128 * G + 8 * lc) * 2) : 0x40000000u;
    if (w != 0) LD4S(KB.r[0], KB.r[1], KB.r[2], KB.r[3], guo, rg, 4 * FF * 2, 8 * FF * 2, 12 * FF * 2);
    SB();
    __syncthreads();
    STAMP(1);
    const u32 tag = __builtin_amdgcn_readfirstlane(E) + 1u;
    if (w == 0) {
        float s0 = 0.f, s1 = 0.f;
#pragma unroll
        for (int ww = 0; ww < 8; ++ww) { const float2 v = *(const float2*)(qpart + ww * 128 + 2 * lane); s0 += v.x; s1 += v.y; }
        v4u u = {__float_as_uint(s0), tag, __float_as_uint(s1), tag};
        __builtin_amdgcn_raw_buffer_store_b128(u, rs, (int)(WS_QP + (b * 64 + lane) * 16), 0, AUX_SC1);
        const unsigned char* qpa = ws + WS_QP + ((G * 8 + (lane >> 3)) * 64 + ks * 8 + (lane & 7)) * 16;
        v4u qa;
        u32 spins = 0;
        for (;;) {
            qa = poll16(qpa);
            if (__all(tagok(qa, tag)) || ++spins > SPIN_CAP) break;
            __builtin_amdgcn_s_sleep(1);
        }
        STAMP(11);
        s0 = __uint_as_float(qa[0]); s1 = __uint_as_float(qa[2]);
        s0 += dpp<ROR8>(s0); s1 += dpp<ROR8>(s1); s0 = xrow_sum(s0); s1 = xrow_sum(s1);
        const u32 pr = cvt1(s0) | (cvt1(s1) << 16);
        const u32 nb = dppu<QXOR1>(pr);
        if (lane < 8 && (lane & 1) == 0) {
            v4u uq = {pr, tag, nb, tag};
            __builtin_amdgcn_raw_buffer_store_b128(uq, rs, (int)(WS_QG + (kvh * 256 + rr * 4 + (lane >> 1)) * 16), 0, AUX_SC1);
        }
        const unsigned char* qp = ws + WS_QG + (kvh * 256 + lane) * 16;
        v4u q0, q1u, q2u, q3u;
        spins = 0;
        for (;;) {
            asm volatile("global_load_dwordx4 %0, %4, off sc1\n\tglobal_load_dwordx4 %1, %4, off offset:1024 sc1\n\tglobal_load_dwordx4 %2, %4, off offset:2048 sc1\n\tglobal_load_dwordx4 %3, %4, off offset:3072 sc1\n\ts_waitcnt vmcnt(0)"
                         : "=&v"(q0), "=&v"(q1u), "=&v"(q2u), "=&v"(q3u) : "v"(qp) : "memory");
            const bool ok = tagok(q0, tag) && tagok(q1u, tag) && tagok(q2u, tag) && tagok(q3u, tag);
            if (__all(ok) || ++spins > SPIN_CAP) break;
            __builtin_amdgcn_s_sleep(1);
        }
        *(v2u*)(qs + lane * 2) = v2u{q0[0], q0[2]};
        *(v2u*)(qs + (64 + lane) * 2) = v2u{q1u[0], q1u[2]};
        *(v2u*)(qs + (128 + lane) * 2) = v2u{q2u[0], q2u[2]};
        *(v2u*)(qs + (192 + lane) * 2) = v2u{q3u[0], q3u[2]};
        asm volatile("s_waitcnt lgkmcnt(0)" ::: "memory");
        *qflag = 1u;
        STAMP(12);
        LD4S(KB.r[0], KB.r[1], KB.r[2], KB.r[3], guo, rg, 4 * FF * 2, 8 * FF * 2, 12 * FF * 2);
    }
    PACE();
    LD4S(KB.r[4], KB.r[5], KB.r[6], KB.r[7], guo, ru, 4 * FF * 2, 8 * FF * 2, 12 * FF * 2); PACE();
    LDK4(KA, 0, to0);
    WAIT8(KB, 4);
    if (has_ffn) {
        float ag[8], au[8];
        gemv_block(&KB.r[0], xv, ag);
        gemv_block(&KB.r[4], xv, au);
        if (lr == 0) {
            float* dg = gpart + w * 128 + 8 * lc; float* du = upart + w * 128 + 8 * lc;
            *(f32x4*)dg = f32x4{ag[0], ag[1], ag[2], ag[3]}; *(f32x4*)(dg + 4) = f32x4{ag[4], ag[5], ag[6], ag[7]};
            *(f32x4*)du = f32x4{au[0], au[1], au[2], au[3]}; *(f32x4*)(du + 4) = f32x4{au[4], au[5], au[6], au[7]};
        }
        asm volatile("s_waitcnt lgkmcnt(0)" ::: "memory");
        u32 old = 0;
        if (lane == 0) old = __hip_atomic_fetch_add((LAS u32*)gucnt, 1u, __ATOMIC_RELAXED, __HIP_MEMORY_SCOPE_WORKGROUP);
        if (__builtin_amdgcn_readfirstlane(old) == 7u) {
            float g0 = 0.f, g1 = 0.f, u0 = 0.f, u1 = 0.f;
#pragma unroll
            for (int ww = 0; ww < 8; ++ww) { const float2 vg = *(const float2*)(gpart + ww * 128 + 2 * lane), vu = *(const float2*)(upart + ww * 128 + 2 * lane); g0 += vg.x; g1 += vg.y; u0 += vu.x; u1 += vu.y; }
            v4u ug = {__float_as_uint(g0), tag, __float_as_uint(g1), tag}, uu_ = {__float_as_uint(u0), tag, __float_as_uint(u1), tag};
            __builtin_amdgcn_raw_buffer_store_b128(ug, rs, (int)(WS_GP + (b * 64 + lane) * 16), 0, AUX_SC1);
            __builtin_amdgcn_raw_buffer_store_b128(uu_, rs, (int)(WS_UP + (b * 64 + lane) * 16), 0, AUX_SC1);
        }
    }
    STAMPW(2, 1);
    PACE();
    LDK4(KA, 1, to0); PACE();
    LDV4(VA, 0, to0); PACE();
    LDV4(VA, 1, to0); PACE();
    LDK4(KB, 0, to1); PACE();
    LDK4(KB, 1, to1); PACE();
    LDV4(VB, 0, to1); PACE();
    LDV4(VB, 1, to1);
    {
        u32 spins = 0;
        while (*qflag == 0u) { __builtin_amdgcn_s_sleep(1); if (++spins > 64u * SPIN_CAP) break; }
        STAMPW(13, 1);
    }
    v4u qf[2];
#pragma unroll
    for (int s = 0; s < 2; ++s) qf[s] = *(const v4u*)(qs + (lane & 7) * 64 + 32 * s + 16 * ((lane >> 3) & 1) + 4 * lr);

    constexpr float C2 = 0.08838834764831845f * 1.4426950408889634f;
    const bool uhi = (lane >> 3) & 1;
    const u32 psh = uhi ? 16u : 0u;
    float m_run = -INFINITY, l_run = 0.f;
    f32x4 acc[8];
#pragma unroll
    for (int d = 0; d < 8; ++d) acc[d] = f32x4{0.f, 0.f, 0.f, 0.f};

    auto qk = [&](const KV& Kb, float (&sc)[4]) {
#pragma unroll
        for (int t = 0; t < 4; ++t) {
            f32x4 c = {0.f, 0.f, 0.f, 0.f};
            c = mfma(Kb.r[2 * t], qf[0], c);
            c = mfma(Kb.r[2 * t + 1], qf[1], c);
            const float t0 = c[0] + dpp<ROR8>(c[1]);
            const float t1 = c[3] + dpp<ROR8>(c[2]);
            sc[t] = uhi ? t1 : t0;
        }
    };
    auto softmax_pv = [&](const float (&sc)[4], const KV& Vb) {
        float mx = fmaxf(fmaxf(sc[0], sc[1]), fmaxf(sc[2], sc[3]));
        mx = fmaxf(mx, dpp<ROR8>(mx));
        mx = xrow_max(mx);
        const float mn = fmaxf(m_run, mx * C2);
        const float alpha = ex2(m_run - mn);
        m_run = mn;
        v4u pf;
        float ps = 0.f;
#pragma unroll
        for (int t = 0; t < 4; ++t) { const float p = ex2(fmaf(sc[t], C2, -mn)); const u32 pb = cvt1(p); ps += bf2f(pb); pf[t] = pb << psh; }
        l_run = fmaf(l_run, alpha, ps);
#pragma unroll
        for (int d = 0; d < 8; ++d) acc[d] *= alpha;
#pragma unroll
        for (int d = 0; d < 8; ++d) {
            v4u a;
#pragma unroll
            for (int q = 0; q < 4; ++q) a[q] = __builtin_amdgcn_perm(Vb.r[2 * q + 1][d >> 1], Vb.r[2 * q][d >> 1], (d & 1) ? 0x07060302u : 0x05040100u);
            acc[d] = mfma(a, pf, acc[d]);
        }
    };
    const int ocol = 128 * w + 8 * lc;
    float sc[4];
    for (int n = 0; n < cnt; n += 2) {
        const u32 ta = toff(n + 2), tb = toff(n + 3);
        WAIT8(KA, 24);
        qk(KA, sc); SB();
        ISSUE_K(KA, ta);
        WAIT8(VA, 24);
        softmax_pv(sc, VA); SB();
        ISSUE_V(VA, ta);
        if (n == 0) STAMP(3);
        const bool vb = n + 1 < cnt;
        WAIT8(KB, 24);
        if (vb) qk(KB, sc);
        SB();
        ISSUE_K(KB, tb);
        WAIT8(VB, 24);
        if (vb) softmax_pv(sc, VB);
        SB();
        ISSUE_V(VB, tb);
        if (n == 0) STAMP(4);
    }
    WAIT8(KA, 0); WAIT8(VA, 0); WAIT8(KB, 0); WAIT8(VB, 0);
    const unsigned char* gpa = ws + WS_GP + ((G * 8 + (lane >> 3)) * 64 + ks * 8 + (lane & 7)) * 16;
    const unsigned char* upa = ws + WS_UP + ((G * 8 + (lane >> 3)) * 64 + ks * 8 + (lane & 7)) * 16;
    v4u gu = {0, 0, 0, 0}, uu = {0, 0, 0, 0};
    if (has_ffn) {
        gu = __builtin_amdgcn_raw_buffer_load_b128(rs, (int)(gpa - ws), 0, AUX_SC1);
        uu = __builtin_amdgcn_raw_buffer_load_b128(rs, (int)(upa - ws), 0, AUX_SC1);
#pragma unroll
        for (int i = 0; i < 4; ++i) VA.r[i] = ldnt(wd + (size_t)(16 * b + 4 * i + lr) * D + ocol);
    } else {
#pragma unroll
        for (int i = 0; i < 4; ++i) VA.r[i] = v4u{0u, 0u, 0u, 0u};
    }
#pragma unroll
    for (int i = 0; i < 4; ++i) VA.r[4 + i] = ldnt(wo + (size_t)(16 * b + 4 * i + lr) * D + ocol);
    SB();
    STAMP(5);
    STAMPW(14, 7);

    {
        l_run += dpp<ROR8>(l_run); l_run = xrow_sum(l_run);
#pragma unroll
        for (int d = 0; d < 8; ++d)
#pragma unroll
            for (int i = 0; i < 4; ++i) acc[d][i] += dpp<ROR8>(acc[d][i]);
        if (!uhi) {
            float* dst = red + (w * 8 + (lane & 7)) * 128 + 32 * lr;
#pragma unroll
            for (int i = 0; i < 4; ++i) {
                *(f32x4*)(dst + 8 * i) = f32x4{acc[0][i], acc[1][i], acc[2][i], acc[3][i]};
                *(f32x4*)(dst + 8 * i + 4) = f32x4{acc[4][i], acc[5][i], acc[6][i], acc[7][i]};
            }
            if (lr == 0) { wml[(w * 8 + (lane & 7)) * 2] = m_run; wml[(w * 8 + (lane & 7)) * 2 + 1] = l_run; }
        }
    }
    __syncthreads();
    {
        float M = -INFINITY;
#pragma unroll
        for (int ww = 0; ww < 8; ++ww) M = fmaxf(M, wml[(ww * 8 + w) * 2]);
        float L = 0.f, o0 = 0.f, o1 = 0.f;
#pragma unroll
        for (int ww = 0; ww < 8; ++ww) {
            const float f = ex2(wml[(ww * 8 + w) * 2] - M);
            L = fmaf(f, wml[(ww * 8 + w) * 2 + 1], L);
            const float2 v = *(const float2*)(red + (ww * 8 + w) * 128 + 2 * lane);
            o0 = fmaf(f, v.x, o0); o1 = fmaf(f, v.y, o1);
        }
        v4u u = {__float_as_uint(o0), tag, __float_as_uint(o1), tag};
        __builtin_amdgcn_raw_buffer_store_b128(u, rs, (int)(WS_P + ((b * 8 + w) * 64 + lane) * 16), 0, AUX_SC1);
        if (lane == 0) { v4u ml = {__float_as_uint(M), tag, __float_as_uint(L), tag};
            __builtin_amdgcn_raw_buffer_store_b128(ml, rs, (int)(WS_ML + (b * 8 + w) * 16), 0, AUX_SC1); }
    }
    STAMP(6);
    const int hq_l = rr >> 3, d0p = (rr & 7) * 8;
    const int src = kvh * 64 + (tid >> 3);
    const int poff = (int)(WS_P + ((src * 8 + hq_l) * 64 + d0p + (tid & 7)) * 16), mloff = (int)(WS_ML + (src * 8 + hq_l) * 16);
    v4u pv = __builtin_amdgcn_raw_buffer_load_b128(rs, poff, 0, AUX_SC1);
    v4u ml = __builtin_amdgcn_raw_buffer_load_b128(rs, mloff, 0, AUX_SC1);
    SB();
    float z = 0.f, r = 0.f;
#pragma unroll
    for (int ww = 0; ww < 8; ++ww) { z += gsc[2 * ww]; r += gsc[2 * ww + 1]; }
    const float g = rbf(sigmoidf_(rbf(z))), sr = rbf(sigmoidf_(rbf(r)));
    const float omg = rbf(1.f - g);
    float h0 = 0.f, h1 = 0.f;
    if (has_ffn) {
        u32 spins = 0;
        while (!__all(tagok(gu, tag) && tagok(uu, tag))) {
            __builtin_amdgcn_s_sleep(1);
            poll16x2(gpa, upa, gu, uu);
            if (++spins > SPIN_CAP) break;
        }
        float g0 = __uint_as_float(gu[0]), g1 = __uint_as_float(gu[2]), u0 = __uint_as_float(uu[0]), u1 = __uint_as_float(uu[2]);
        g0 += dpp<ROR8>(g0); g1 += dpp<ROR8>(g1); u0 += dpp<ROR8>(u0); u1 += dpp<ROR8>(u1);
        g0 = rbf(xrow_sum(g0)); g1 = rbf(xrow_sum(g1)); u0 = rbf(xrow_sum(u0)); u1 = rbf(xrow_sum(u1));
        h0 = rbf(rbf(g0 * sigmoidf_(g0)) * u0) * (omg * sr);
        h1 = rbf(rbf(g1 * sigmoidf_(g1)) * u1) * (omg * sr);
    }
    float a[8];
#pragma unroll
    for (int k = 0; k < 8; ++k) a[k] = 0.f;
#pragma unroll
    for (int i = 0; i < 4; ++i) {
        const int row = 4 * i + lr;
        const float c0 = __shfl(h0, row >> 1), c1 = __shfl(h1, row >> 1);
        const float ch = (row & 1) ? c1 : c0;
#pragma unroll
        for (int k = 0; k < 4; ++k) { a[2 * k] = fmaf(ch, bflo(VA.r[i][k]), a[2 * k]); a[2 * k + 1] = fmaf(ch, bfhi(VA.r[i][k]), a[2 * k + 1]); }
    }
    STAMP(7);
    {
        u32 spins = 0;
        while (!__all(tagok(pv, tag) && tagok(ml, tag))) {
            __builtin_amdgcn_s_sleep(1);
            poll16x2(ws + poff, ws + mloff, pv, ml);
            if (++spins > SPIN_CAP) break;
        }
        const float ms = __uint_as_float(ml[0]), ls = __uint_as_float(ml[2]);
        float Mw = fmaxf(ms, dpp<ROR8>(ms)); Mw = xrow_max(Mw);
        const float e = ex2(ms - Mw);
        float L = e * ls, o0 = e * __uint_as_float(pv[0]), o1 = e * __uint_as_float(pv[2]);
        L += dpp<ROR8>(L); o0 += dpp<ROR8>(o0); o1 += dpp<ROR8>(o1);
        L = xrow_sum(L); o0 = xrow_sum(o0); o1 = xrow_sum(o1);
        if (lane < 8) *(float2*)(cw + w * 16 + 2 * lane) = make_float2(o0, o1);
        if (lane == 0) { cml[2 * w] = Mw; cml[2 * w + 1] = L; }
    }
    __syncthreads();
    STAMP(8);
    {
        float M = -INFINITY;
#pragma unroll
        for (int ww = 0; ww < 8; ++ww) M = fmaxf(M, cml[2 * ww]);
        float L = 0.f, o = 0.f;
#pragma unroll
        for (int ww = 0; ww < 8; ++ww) { const float f = ex2(cml[2 * ww] - M); L = fmaf(f, cml[2 * ww + 1], L); o = fmaf(f, cw[ww * 16 + lc], o); }
        const float ov = rbf(o / L) * g;
#pragma unroll
        for (int i = 0; i < 4; ++i) {
            const float co = __shfl(ov, 4 * i + lr);
#pragma unroll
            for (int k = 0; k < 4; ++k) { a[2 * k] = fmaf(co, bflo(VA.r[4 + i][k]), a[2 * k]); a[2 * k + 1] = fmaf(co, bfhi(VA.r[4 + i][k]), a[2 * k + 1]); }
        }
#pragma unroll
        for (int k = 0; k < 8; ++k) a[k] = xrow_sum(a[k]);
        const float v0 = lr == 0 ? a[0] : lr == 1 ? a[2] : lr == 2 ? a[4] : a[6];
        const float v1 = lr == 0 ? a[1] : lr == 1 ? a[3] : lr == 2 ? a[5] : a[7];
        v4u u = {__float_as_uint(v0), tag, __float_as_uint(v1), tag};
        __builtin_amdgcn_raw_buffer_store_b128(u, rs, (int)(WS_OUTP + (b * 512 + (ocol >> 1) + lr) * 16), 0, AUX_SC1);
        STAMP(9);
        const unsigned char* fp = ws + WS_OUTP + ((tid >> 1) * 512 + 2 * b + (tid & 1)) * 16;
        v4u fv;
        u32 spins = 0;
        for (;;) {
            fv = poll16(fp);
            if (__all(tagok(fv, tag)) || ++spins > SPIN_CAP) break;
            __builtin_amdgcn_s_sleep(1);
        }
        STAMP(10);
        float f0 = __uint_as_float(fv[0]), f1 = __uint_as_float(fv[2]);
        f0 += dpp<ROR2>(f0); f0 += dpp<ROR4>(f0); f0 += dpp<ROR8>(f0); f0 = xrow_sum(f0);
        f1 += dpp<ROR2>(f1); f1 += dpp<ROR4>(f1); f1 += dpp<ROR8>(f1); f1 = xrow_sum(f1);
        if (lane < 2) { fin[w * 4 + 2 * lane] = f0; fin[w * 4 + 2 * lane + 1] = f1; }
        __syncthreads();
        if (tid < 4) {
            float s = 0.f;
#pragma unroll
            for (int ww = 0; ww < 8; ++ww) s += fin[ww * 4 + tid];
            const float xo = bf2f(x[4 * b + tid]);
            out[4 * b + tid] = (u16)cvt1(s + omg * xo);
        }
        if (b == 0 && tid == 0) __hip_atomic_store((GAS u32*)(ws + WS_EPOCH), tag, __ATOMIC_RELAXED, __HIP_MEMORY_SCOPE_AGENT);
    }
    STAMP(15);
    __syncthreads();
    if (tid == 0 && (b % 37 == 0 || b == 255)) {
        __builtin_amdgcn_s_waitcnt(0);
        const volatile GAS u64* st = (const volatile GAS u64*)(ws + STAMP_OFF) + b * NSTAMP;
        const u64 t0 = st[0];
        printf("WG %3d t0 %llu : barA %.2f gu %.2f pollA %.2f pollB %.2f flag %.2f tile0 %.2f tile1 %.2f attn0 %.2f attn7 %.2f pub %.2f h %.2f comb %.2f outp %.2f fin %.2f end %.2f\n", b, (unsigned long long)(t0 % 100000000ull),
            (st[1] - t0) * 0.01, (st[2] - t0) * 0.01, (st[11] - t0) * 0.01, (st[12] - t0) * 0.01, (st[13] - t0) * 0.01, (st[3] - t0) * 0.01, (st[4] - t0) * 0.01, (st[5] - t0) * 0.01, (st[14] - t0) * 0.01,
            (st[6] - t0) * 0.01, (st[7] - t0) * 0.01, (st[8] - t0) * 0.01, (st[9] - t0) * 0.01, (st[10] - t0) * 0.01, (st[15] - t0) * 0.01);
    }
}

extern "C" void kernel_launch(void* const* d_in, const int* in_sizes, int n_in, void* d_out, int out_size, void* d_ws, size_t ws_size, hipStream_t stream) {
    const u16* x = (const u16*)d_in[0]; const u16* kc = (const u16*)d_in[1]; const u16* vc = (const u16*)d_in[2];
    const u16* wq = (const u16*)d_in[3]; const u16* wo = (const u16*)d_in[4];
    const u16* wr = (const u16*)d_in[6]; const u16* wg = (const u16*)d_in[7]; const u16* wu = (const u16*)d_in[8]; const u16* wd = (const u16*)d_in[9]; const u16* gw = (const u16*)d_in[10];
    fused_decode<<<NWG, NTHR, 0, stream>>>(x, kc, vc, wq, wo, wr, wg, wu, wd, gw, (u16*)d_out, (unsigned char*)d_ws);
}
```

```cpp
#include <hip/hip_runtime.h>
#include <hip/hip_bf16.h>
#include <cstdint>

typedef unsigned short u16;
typedef unsigned int u32;
typedef unsigned long long u64;
typedef u32 v4u __attribute__((ext_vector_type(4)));
typedef u32 v2u __attribute__((ext_vector_type(2)));
typedef float f32x4 __attribute__((ext_vector_type(4)));
typedef __bf16 bf16x8 __attribute__((ext_vector_type(8)));
#define GAS __attribute__((address_space(1)))
#define LAS __attribute__((address_space(3)))

#define PACE_N 63
#define TK_TGT 0
#define RESERVE_AT 2
#define NSW 4
#define W_MODE 0

namespace {
constexpr int D = 1024, SK = 65536, DH = 128, FF = 3584;
constexpr int NWG = 256, NTHR = 512;
constexpr u32 NSTAT = 56 * NSW, NDYN = 256 - NSTAT;
constexpr u32 OOB = 0x40000000u;

constexpr u32 WS_EPOCH = 0;
constexpr u32 WS_CTR = 4096;
constexpr u32 WS_QG = WS_CTR + 32 * 256;
constexpr u32 WS_QP = WS_QG + 4 * 256 * 16;
constexpr u32 WS_GP = WS_QP + 256 * 64 * 16;
constexpr u32 WS_UP = WS_GP + 256 * 64 * 16;
constexpr u32 WS_ML = WS_UP + 256 * 64 * 16;
constexpr u32 WS_P = WS_ML + 256 * 8 * 16;
constexpr u32 WS_OUTP = WS_P + 256 * 8 * 64 * 16;
constexpr u32 WS_END = WS_OUTP + 256 * 512 * 16;
constexpr int AUX_SC1 = 16;
constexpr u32 SPIN_CAP = 1u << 18;

#define STAMP(k) do { } while (0)
#define STAMPW(k, wv_) do { } while (0)

__device__ __forceinline__ float bflo(u32 w) { return __builtin_bit_cast(float, w << 16); }
__device__ __forceinline__ float bfhi(u32 w) { return __builtin_bit_cast(float, w & 0xffff0000u); }
__device__ __forceinline__ float bf2f(u32 b) { return __builtin_bit_cast(float, b << 16); }
__device__ __forceinline__ u32 cvt1(float v) { return (u32)__builtin_bit_cast(u16, (__bf16)v); }
__device__ __forceinline__ float rbf(float f) { return bf2f(cvt1(f)); }
template <int CTRL> __device__ __forceinline__ float dpp(float x) {
    return __builtin_bit_cast(float, __builtin_amdgcn_mov_dpp(__builtin_bit_cast(int, x), CTRL, 0xf, 0xf, true));
}
template <int CTRL> __device__ __forceinline__ u32 dppu(u32 x) { return (u32)__builtin_amdgcn_mov_dpp((int)x, CTRL, 0xf, 0xf, true); }
constexpr int ROR1 = 0x121, ROR2 = 0x122, ROR4 = 0x124, ROR8 = 0x128, QXOR1 = 0xB1;
__device__ __forceinline__ float xrow_sum(float x) {
    auto s = __builtin_amdgcn_permlane16_swap(__float_as_uint(x), __float_as_uint(x), false, false);
    x = __uint_as_float(s[0]) + __uint_as_float(s[1]);
    auto t = __builtin_amdgcn_permlane32_swap(__float_as_uint(x), __float_as_uint(x), false, false);
    return __uint_as_float(t[0]) + __uint_as_float(t[1]);
}
__device__ __forceinline__ float xrow_max(float x) {
    auto s = __builtin_amdgcn_permlane16_swap(__float_as_uint(x), __float_as_uint(x), false, false);
    x = fmaxf(__uint_as_float(s[0]), __uint_as_float(s[1]));
    auto t = __builtin_amdgcn_permlane32_swap(__float_as_uint(x), __float_as_uint(x), false, false);
    return fmaxf(__uint_as_float(t[0]), __uint_as_float(t[1]));
}
__device__ __forceinline__ f32x4 mfma(v4u a, v4u b, f32x4 c) {
    return __builtin_amdgcn_mfma_f32_16x16x32_bf16(__builtin_bit_cast(bf16x8, a), __builtin_bit_cast(bf16x8, b), c, 0, 0, 0);
}
__device__ __forceinline__ v4u ldnt(const void* p) { return __builtin_nontemporal_load((const GAS v4u*)p); }
__device__ __forceinline__ float ex2(float x) { return __builtin_amdgcn_exp2f(x); }
__device__ __forceinline__ float sigmoidf_(float z) { return 1.f / (1.f + __expf(-z)); }
#define SB() __builtin_amdgcn_sched_barrier(0)
__device__ __forceinline__ v4u poll16(const unsigned char* p) {
    v4u r;
    asm volatile("global_load_dwordx4 %0, %1, off sc1\n\ts_waitcnt vmcnt(0)" : "=&v"(r) : "v"(p) : "memory");
    return r;
}
__device__ __forceinline__ void poll16x2(const unsigned char* p, const unsigned char* q, v4u& a, v4u& b) {
    asm volatile("global_load_dwordx4 %0, %2, off sc1\n\tglobal_load_dwordx4 %1, %3, off sc1\n\ts_waitcnt vmcnt(0)" : "=&v"(a), "=&v"(b) : "v"(p), "v"(q) : "memory");
}
__device__ __forceinline__ bool tagok(const v4u& u, u32 tag) { return u[1] == tag && u[3] == tag; }

struct KV { v4u r[8]; };
__device__ __forceinline__ void gemv_block(const v4u* wv, const float (&xv)[4], float (&a)[8]) {
#pragma unroll
    for (int k = 0; k < 8; ++k) a[k] = 0.f;
#pragma unroll
    for (int i = 0; i < 4; ++i)
#pragma unroll
        for (int k = 0; k < 4; ++k) { a[2 * k] = fmaf(xv[i], bflo(wv[i][k]), a[2 * k]); a[2 * k + 1] = fmaf(xv[i], bfhi(wv[i][k]), a[2 * k + 1]); }
#pragma unroll
    for (int k = 0; k < 8; ++k) a[k] = xrow_sum(a[k]);
}
}

#define LD4(R0, R1, R2, R3, VO, RS, O1, O2, O3) asm volatile( \
        "buffer_load_dwordx4 %0, %4, %5, 0 offen nt\n\tbuffer_load_dwordx4 %1, %4, %5, 0 offen offset:" #O1 " nt\n\t" \
        "buffer_load_dwordx4 %2, %4, %5, 0 offen offset:" #O2 " nt\n\tbuffer_load_dwordx4 %3, %4, %5, 0 offen offset:" #O3 " nt" \
        : "=&v"(R0), "=&v"(R1), "=&v"(R2), "=&v"(R3) : "v"(VO), "s"(RS))
#define LD4S(R0, R1, R2, R3, VO, RS, S1, S2, S3) asm volatile( \
        "buffer_load_dwordx4 %0, %4, %5, 0 offen nt\n\tbuffer_load_dwordx4 %1, %4, %5, %6 offen nt\n\t" \
        "buffer_load_dwordx4 %2, %4, %5, %7 offen nt\n\tbuffer_load_dwordx4 %3, %4, %5, %8 offen nt" \
        : "=&v"(R0), "=&v"(R1), "=&v"(R2), "=&v"(R3) : "v"(VO), "s"(RS), "s"(S1), "s"(S2), "s"(S3))
#define LDK4(BUF, H, TO) LD4(BUF.r[4 * (H)], BUF.r[4 * (H) + 1], BUF.r[4 * (H) + 2], BUF.r[4 * (H) + 3], kvo + (TO) + 4096u * (H), rk, 128, 2048, 2176)
#define LDV4(BUF, H, TO) LD4(BUF.r[4 * (H)], BUF.r[4 * (H) + 1], BUF.r[4 * (H) + 2], BUF.r[4 * (H) + 3], vvo + (TO) + 4096u * (H), rv, 1024, 2048, 3072)
#define ISSUE_K(BUF, TO) do { LDK4(BUF, 0, TO); LDK4(BUF, 1, TO); } while (0)
#define ISSUE_V(BUF, TO) do { LDV4(BUF, 0, TO); LDV4(BUF, 1, TO); } while (0)
#define ISSUE_W() do { int l_ = __builtin_amdgcn_mbcnt_hi(~0u, __builtin_amdgcn_mbcnt_lo(~0u, 0u)); asm volatile("" : "+v"(l_)); \
                       const u32 woo_ = (u32)(((16 * b + (l_ >> 4)) * D + 128 * w + 8 * (l_ & 15)) * 2), wdo_ = has_ffn ? woo_ : OOB; \
                       _Pragma("unroll") for (int i_ = 0; i_ < 4; ++i_) DMA1(ldsbase + wslot + 1024u * i_, wdo_, rd, (u32)(8192 * i_)); \
                       _Pragma("unroll") for (int i_ = 0; i_ < 4; ++i_) DMA1(ldsbase + wslot + 4096u + 1024u * i_, woo_, ro, (u32)(8192 * i_)); } while (0)
#define DMA1(LDSA, VO, RS, SO) asm volatile("s_mov_b32 m0, %0\n\ts_nop 0\n\tbuffer_load_dwordx4 %1, %2, %3 offen nt lds" :: "s"(LDSA), "v"(VO), "s"(RS), "s"(SO) : "m0", "memory")
#define DMA_K(LDSA, TO) do { _Pragma("unroll") for (int i_ = 0; i_ < 8; ++i_) DMA1((LDSA) + 1024u * i_, kvo + (TO), rk, (u32)((i_ >> 1) * 2048 + (i_ & 1) * 128)); } while (0)
#define DMA_V(LDSA, TO) do { _Pragma("unroll") for (int i_ = 0; i_ < 8; ++i_) DMA1((LDSA) + 8192u + 1024u * i_, vvo + (TO), rv, (u32)(i_ * 1024)); } while (0)
#define WAIT8(BUF, N) asm volatile("s_waitcnt vmcnt(%8)" : "+v"(BUF.r[0]), "+v"(BUF.r[1]), "+v"(BUF.r[2]), "+v"(BUF.r[3]), "+v"(BUF.r[4]), "+v"(BUF.r[5]), "+v"(BUF.r[6]), "+v"(BUF.r[7]) : "n"(N))
#define PACE() do { SB(); if (__builtin_amdgcn_readfirstlane(*qflag) == 0u) asm volatile("s_waitcnt vmcnt(%0)" :: "n"(PACE_N) : "memory"); SB(); } while (0)

__global__ void __launch_bounds__(NTHR, 2)
fused_decode(const u16* __restrict__ x, const u16* __restrict__ kc, const u16* __restrict__ vc, const u16* __restrict__ wq, const u16* __restrict__ wo,
             const u16* __restrict__ wr, const u16* __restrict__ wg, const u16* __restrict__ wu, const u16* __restrict__ wd, const u16* __restrict__ gw,
             u16* __restrict__ out, unsigned char* ws)
{
    const int tid = threadIdx.x, lane = tid & 63, w = __builtin_amdgcn_readfirstlane(tid >> 6);
    __shared__ __attribute__((aligned(16))) float lds[7 * 4096 + 3 * 1024 + 512 + 512 + 2048];
    float* gpart = lds + 7 * 4096;
    float* upart = lds + 7 * 4096 + 1024;
    float* qpart = lds + 7 * 4096 + 2048;
    float* sm = lds + 7 * 4096 + 3072;
    u32* qs = (u32*)(lds + 7 * 4096 + 3072 + 512);
    const u32 wslot = w == 0 ? (u32)((7 * 4096 + 3 * 1024 + 1024) * 4) : (u32)(w - 1) * 16384u + 8192u;
    float* gsc = sm;
    float* wml = sm + 32;
    float* cw = sm + 160;
    float* cml = sm + 288;
    float* fin = sm + 320;
    volatile LAS u32* tk_ring = (volatile LAS u32*)(sm + 448);
    volatile LAS u32* qflag = (volatile LAS u32*)(sm + 480);
    volatile LAS u32* gucnt = (volatile LAS u32*)(sm + 481);
    volatile LAS u32* tk_head = (volatile LAS u32*)(sm + 482);
    volatile LAS u32* tk_tail = (volatile LAS u32*)(sm + 483);
    float* gts = sm + 484;
    float* hs = sm + 488;

    const int b = blockIdx.x, kvh = b >> 6, rr = b & 63, G = b >> 3, ks = b & 7;
    const __amdgpu_buffer_rsrc_t rs = __builtin_amdgcn_make_buffer_rsrc((void*)ws, 0, (int)WS_END, 0x00020000);
    const bool has_ffn = G < 28;
    const int lc = lane & 15, lr = lane >> 4;
    const int gi = G & 7, pj = (ks - gi) & 7;
    STAMP(0);

    const u32 E = __hip_atomic_load((GAS u32*)(ws + WS_EPOCH), __ATOMIC_RELAXED, __HIP_MEMORY_SCOPE_AGENT);
    const int grow = 128 * ks + 16 * w + lr;
    u32 xr[4];
#pragma unroll
    for (int i = 0; i < 4; ++i) xr[i] = x[grow + 4 * i];
    v4u wv[4];
#pragma unroll
    for (int i = 0; i < 4; ++i) wv[i] = ldnt(wq + (size_t)(grow + 4 * i) * 4096 + 128 * G + 8 * lc);
    const u32 xp = ((const u32*)x)[tid], gp = ((const u32*)gw)[tid], rp = ((const u32*)wr)[tid];
    if (tid == 0) { *qflag = 0u; *gucnt = 0u; *tk_head = 0u; *tk_tail = 0u; }
    SB();
    const __amdgpu_buffer_rsrc_t rk = __builtin_amdgcn_make_buffer_rsrc((void*)(kc + (size_t)kvh * SK * DH), 0, SK * DH * 2, 0x00020000);
    const __amdgpu_buffer_rsrc_t rv = __builtin_amdgcn_make_buffer_rsrc((void*)(vc + (size_t)kvh * SK * DH), 0, SK * DH * 2, 0x00020000);
    const __amdgpu_buffer_rsrc_t rg = __builtin_amdgcn_make_buffer_rsrc((void*)wg, 0, D * FF * 2, 0x00020000);
    const __amdgpu_buffer_rsrc_t ru = __builtin_amdgcn_make_buffer_rsrc((void*)wu, 0, D * FF * 2, 0x00020000);
    const __amdgpu_buffer_rsrc_t rd = __builtin_amdgcn_make_buffer_rsrc((void*)wd, 0, FF * D * 2, 0x00020000);
    const __amdgpu_buffer_rsrc_t ro = __builtin_amdgcn_make_buffer_rsrc((void*)wo, 0, 4096 * D * 2, 0x00020000);
    const u32 kvo = (u32)((4 * ((lc >> 1) & 1) + (lc >> 2)) * 256 + (lc & 1) * 64 + lr * 16);
    const u32 vvo = (u32)(lane * 16);
    const int ocol = 128 * w + 8 * lc;
    const u32 tbase = (u32)(32 * rr), ntw = 32u;
    const u32 to0 = (tbase + (u32)(w - 1)) * 8192u;
    constexpr u32 TS = 7u * 8192u;
    const u32 to4 = (u32)(w - 1) + 28u < ntw ? to0 + 4 * TS : OOB;
    KV KA, VA, KB, VB;
    const u32 ldsbase = (u32)(size_t)(LAS void*)lds;

    float xv[4];
#pragma unroll
    for (int i = 0; i < 4; ++i) xv[i] = bf2f(xr[i]);
    {
        float a[8];
        gemv_block(wv, xv, a);
        if (lr == 0) { float* dst = qpart + w * 128 + 8 * lc; *(f32x4*)dst = f32x4{a[0], a[1], a[2], a[3]}; *(f32x4*)(dst + 4) = f32x4{a[4], a[5], a[6], a[7]}; }
        float z = rbf(bflo(xp) * bflo(gp)) + rbf(bfhi(xp) * bfhi(gp));
        float r = bflo(xp) * bflo(rp) + bfhi(xp) * bfhi(rp);
        z += dpp<ROR1>(z); z += dpp<ROR2>(z); z += dpp<ROR4>(z); z += dpp<ROR8>(z); z = xrow_sum(z);
        r += dpp<ROR1>(r); r += dpp<ROR2>(r); r += dpp<ROR4>(r); r += dpp<ROR8>(r); r = xrow_sum(r);
        if (lane == 0) { gsc[2 * w] = z; gsc[2 * w + 1] = r; }
    }
    const u32 guo = has_ffn ? (u32)((grow * FF + 128 * G + 8 * lc) * 2) : OOB;
    if (w != 0) LD4S(KB.r[0], KB.r[1], KB.r[2], KB.r[3], guo, rg, 4 * FF * 2, 8 * FF * 2, 12 * FF * 2);
    SB();
    __syncthreads();
    STAMP(1);
    const u32 tag = __builtin_amdgcn_readfirstlane(E) + 1u;
    auto gate_up = [&]() {
        if (has_ffn) {
            float ag[8], au[8];
            gemv_block(&KB.r[0], xv, ag);
            gemv_block(&KB.r[4], xv, au);
            if (lr == 0) {
                float* dg = gpart + w * 128 + 8 * lc; float* du = upart + w * 128 + 8 * lc;
                *(f32x4*)dg = f32x4{ag[0], ag[1], ag[2], ag[3]}; *(f32x4*)(dg + 4) = f32x4{ag[4], ag[5], ag[6], ag[7]};
                *(f32x4*)du = f32x4{au[0], au[1], au[2], au[3]}; *(f32x4*)(du + 4) = f32x4{au[4], au[5], au[6], au[7]};
            }
            asm volatile("s_waitcnt lgkmcnt(0)" ::: "memory");
            u32 old = 0;
            if (lane == 0) old = __hip_atomic_fetch_add((LAS u32*)gucnt, 1u, __ATOMIC_RELAXED, __HIP_MEMORY_SCOPE_WORKGROUP);
            if (__builtin_amdgcn_readfirstlane(old) == 7u) {
                float g0 = 0.f, g1 = 0.f, u0 = 0.f, u1 = 0.f;
#pragma unroll
                for (int ww = 0; ww < 8; ++ww) { const float2 vg = *(const float2*)(gpart + ww * 128 + 2 * lane), vu = *(const float2*)(upart + ww * 128 + 2 * lane); g0 += vg.x; g1 += vg.y; u0 += vu.x; u1 += vu.y; }
                v4u ug = {__float_as_uint(g0), tag, __float_as_uint(g1), tag}, uu_ = {__float_as_uint(u0), tag, __float_as_uint(u1), tag};
                __builtin_amdgcn_raw_buffer_store_b128(ug, rs, (int)(WS_GP + (b * 64 + lane) * 16), 0, AUX_SC1);
                __builtin_amdgcn_raw_buffer_store_b128(uu_, rs, (int)(WS_UP + (b * 64 + lane) * 16), 0, AUX_SC1);
            }
        }
    };

    constexpr float C2 = 0.08838834764831845f * 1.4426950408889634f;
    const bool uhi = (lane >> 3) & 1;
    const u32 psh = uhi ? 16u : 0u;
    float m_run = -INFINITY, l_run = 0.f;
    f32x4 acc[8];
#pragma unroll
    for (int d = 0; d < 8; ++d) acc[d] = f32x4{0.f, 0.f, 0.f, 0.f};
    v4u qf[2];
    float sc[4];
    auto qk = [&](const KV& Kb) {
#pragma unroll
        for (int t = 0; t < 4; ++t) {
            f32x4 c = {0.f, 0.f, 0.f, 0.f};
            c = mfma(Kb.r[2 * t], qf[0], c);
            c = mfma(Kb.r[2 * t + 1], qf[1], c);
            const float t0 = c[0] + dpp<ROR8>(c[1]);
            const float t1 = c[3] + dpp<ROR8>(c[2]);
            sc[t] = uhi ? t1 : t0;
        }
    };
    auto softmax_pv = [&](const KV& Vb) {
        float mx = fmaxf(fmaxf(sc[0], sc[1]), fmaxf(sc[2], sc[3]));
        mx = fmaxf(mx, dpp<ROR8>(mx));
        mx = xrow_max(mx);
        const float mn = fmaxf(m_run, mx * C2);
        const float alpha = ex2(m_run - mn);
        m_run = mn;
        v4u pf;
        float ps = 0.f;
#pragma unroll
        for (int t = 0; t < 4; ++t) { const float p = ex2(fmaf(sc[t], C2, -mn)); const u32 pb = cvt1(p); ps += bf2f(pb); pf[t] = pb << psh; }
        l_run = fmaf(l_run, alpha, ps);
#pragma unroll
        for (int d = 0; d < 8; ++d) acc[d] *= alpha;
#pragma unroll
        for (int d = 0; d < 8; ++d) {
            v4u a;
#pragma unroll
            for (int q = 0; q < 4; ++q) a[q] = __builtin_amdgcn_perm(Vb.r[2 * q + 1][d >> 1], Vb.r[2 * q][d >> 1], (d & 1) ? 0x07060302u : 0x05040100u);
            acc[d] = mfma(a, pf, acc[d]);
        }
    };

    if (w == 0) {
        {
            float s0 = 0.f, s1 = 0.f;
#pragma unroll
            for (int ww = 0; ww < 8; ++ww) { const float2 v = *(const float2*)(qpart + ww * 128 + 2 * lane); s0 += v.x; s1 += v.y; }
            v4u u = {__float_as_uint(s0), tag, __float_as_uint(s1), tag};
            __builtin_amdgcn_raw_buffer_store_b128(u, rs, (int)(WS_QP + (b * 64 + lane) * 16), 0, AUX_SC1);
        }
        const int uo = ((G * 8 + (lane >> 3)) * 64 + ks * 8 + (lane & 7)) * 16;
        v4u qa;
        u32 spins = 0;
        for (;;) {
            qa = poll16(ws + WS_QP + uo);
            if (__all(tagok(qa, tag)) || ++spins > SPIN_CAP) break;
            __builtin_amdgcn_s_sleep(1);
        }
        STAMP(11);
        float s0 = __uint_as_float(qa[0]), s1 = __uint_as_float(qa[2]);
        s0 += dpp<ROR8>(s0); s1 += dpp<ROR8>(s1); s0 = xrow_sum(s0); s1 = xrow_sum(s1);
        const u32 pr = cvt1(s0) | (cvt1(s1) << 16);
        const u32 nb = dppu<QXOR1>(pr);
        if (lane < 8 && (lane & 1) == 0) {
            v4u uq = {pr, tag, nb, tag};
            __builtin_amdgcn_raw_buffer_store_b128(uq, rs, (int)(WS_QG + (kvh * 256 + rr * 4 + (lane >> 1)) * 16), 0, AUX_SC1);
        }
        const unsigned char* qp = ws + WS_QG + (kvh * 256 + lane) * 16;
        v4u q0, q1u, q2u, q3u;
        spins = 0;
        for (;;) {
            asm volatile("global_load_dwordx4 %0, %4, off sc1\n\tglobal_load_dwordx4 %1, %4, off offset:1024 sc1\n\tglobal_load_dwordx4 %2, %4, off offset:2048 sc1\n\tglobal_load_dwordx4 %3, %4, off offset:3072 sc1\n\ts_waitcnt vmcnt(0)"
                         : "=&v"(q0), "=&v"(q1u), "=&v"(q2u), "=&v"(q3u) : "v"(qp) : "memory");
            const bool ok = tagok(q0, tag) && tagok(q1u, tag) && tagok(q2u, tag) && tagok(q3u, tag);
            if (__all(ok) || ++spins > SPIN_CAP) break;
            __builtin_amdgcn_s_sleep(1);
        }
        *(v2u*)(qs + lane * 2) = v2u{q0[0], q0[2]};
        *(v2u*)(qs + (64 + lane) * 2) = v2u{q1u[0], q1u[2]};
        *(v2u*)(qs + (128 + lane) * 2) = v2u{q2u[0], q2u[2]};
        *(v2u*)(qs + (192 + lane) * 2) = v2u{q3u[0], q3u[2]};
        asm volatile("s_waitcnt lgkmcnt(0)" ::: "memory");
        *qflag = 1u;
        STAMP(12);
        LD4S(KB.r[0], KB.r[1], KB.r[2], KB.r[3], guo, rg, 4 * FF * 2, 8 * FF * 2, 12 * FF * 2);
        LD4S(KB.r[4], KB.r[5], KB.r[6], KB.r[7], guo, ru, 4 * FF * 2, 8 * FF * 2, 12 * FF * 2);
        ISSUE_W();
        WAIT8(KB, 8);
        gate_up();
        float z = 0.f, r = 0.f;
#pragma unroll
        for (int ww = 0; ww < 8; ++ww) { z += gsc[2 * ww]; r += gsc[2 * ww + 1]; }
        const float g = rbf(sigmoidf_(rbf(z))), sr = rbf(sigmoidf_(rbf(r)));
        const float omg = rbf(1.f - g);
        float h0 = 0.f, h1 = 0.f;
        if (has_ffn) {
            v4u gu, uu;
            spins = 0;
            for (;;) {
                poll16x2(ws + WS_GP + uo, ws + WS_UP + uo, gu, uu);
                if (__all(tagok(gu, tag) && tagok(uu, tag)) || ++spins > SPIN_CAP) break;
                __builtin_amdgcn_s_sleep(1);
            }
            float g0 = __uint_as_float(gu[0]), g1 = __uint_as_float(gu[2]), u0 = __uint_as_float(uu[0]), u1 = __uint_as_float(uu[2]);
            g0 += dpp<ROR8>(g0); g1 += dpp<ROR8>(g1); u0 += dpp<ROR8>(u0); u1 += dpp<ROR8>(u1);
            g0 = rbf(xrow_sum(g0)); g1 = rbf(xrow_sum(g1)); u0 = rbf(xrow_sum(u0)); u1 = rbf(xrow_sum(u1));
            h0 = rbf(rbf(g0 * sigmoidf_(g0)) * u0) * (omg * sr);
            h1 = rbf(rbf(g1 * sigmoidf_(g1)) * u1) * (omg * sr);
        }
        if (lane < 8) *(float2*)(hs + 2 * lane) = make_float2(h0, h1);
        if (lane == 0) { gts[0] = g; gts[1] = omg; }
    } else {
        LD4S(KB.r[4], KB.r[5], KB.r[6], KB.r[7], guo, ru, 4 * FF * 2, 8 * FF * 2, 12 * FF * 2);
        LDK4(KA, 0, to0);
        LDK4(KA, 1, to0);
        WAIT8(KB, 8);
        gate_up();
        STAMPW(2, 1);
        const u32 slot = ldsbase + (u32)(w - 1) * 16384u;
        LDV4(VA, 0, to0); PACE();
        LDV4(VA, 1, to0); PACE();
        LDK4(KB, 0, to0 + TS); PACE();
        LDK4(KB, 1, to0 + TS); PACE();
        LDV4(VB, 0, to0 + TS); PACE();
        LDV4(VB, 1, to0 + TS); PACE();
        DMA_K(slot, to0 + 2 * TS); PACE();
        DMA_V(slot, to0 + 2 * TS);
        {
            u32 spins = 0;
            while (*qflag == 0u) { __builtin_amdgcn_s_sleep(1); if (++spins > 64u * SPIN_CAP) break; }
            STAMPW(13, 1);
        }
#pragma unroll
        for (int s = 0; s < 2; ++s) qf[s] = *(const v4u*)(qs + (lane & 7) * 64 + 32 * s + 16 * ((lane >> 3) & 1) + 4 * lr);
        WAIT8(KA, 40); qk(KA); SB();
        ISSUE_K(KA, to0 + 3 * TS);
        WAIT8(VA, 40); softmax_pv(VA); SB();
        ISSUE_V(VA, to0 + 3 * TS);
        STAMPW(3, 1);
        WAIT8(KB, 40); qk(KB); SB();
        WAIT8(VB, 32); softmax_pv(VB); SB();
        STAMPW(4, 1);
        constexpr int WE = W_MODE == 2 ? 8 : 0;
        asm volatile("s_waitcnt vmcnt(16)" ::: "memory");
        STAMPW(15, 1);
        {
            const LAS v4u* sl = (const LAS v4u*)(lds + (w - 1) * 4096) + lane;
#pragma unroll
            for (int i = 0; i < 8; ++i) { KB.r[i] = sl[64 * i]; VB.r[i] = sl[512 + 64 * i]; }
            qk(KB); SB();
            ISSUE_K(KB, to4);
            softmax_pv(VB); SB();
            ISSUE_V(VB, to4);
        }
        if (W_MODE == 2) ISSUE_W();
        WAIT8(KA, 24 + WE);
        STAMPW(16, 1);
        qk(KA); SB();
        WAIT8(VA, 16 + WE); softmax_pv(VA); SB();
        STAMPW(17, 1);
        WAIT8(KB, 8 + WE);
        if (to4 != OOB) qk(KB);
        SB();
        WAIT8(VB, WE);
        if (to4 != OOB) softmax_pv(VB);
        SB();
        if (W_MODE == 1) ISSUE_W();
    }
    STAMP(5);
    STAMPW(14, 7); STAMPW(18, 1);

    {
    int lane_ = __builtin_amdgcn_mbcnt_hi(~0u, __builtin_amdgcn_mbcnt_lo(~0u, 0u));
    asm volatile("" : "+v"(lane_));
    const int lane = lane_, tid = w * 64 + lane, lc = lane & 15, lr = lane >> 4, ocol = 128 * w + 8 * lc;
    const bool uhi = (lane >> 3) & 1;
    {
        l_run += dpp<ROR8>(l_run); l_run = xrow_sum(l_run);
#pragma unroll
        for (int d = 0; d < 8; ++d)
#pragma unroll
            for (int i = 0; i < 4; ++i) acc[d][i] += dpp<ROR8>(acc[d][i]);
        if (!uhi && w != 0) {
            float* dst = lds + (w - 1) * 4096 + (lane & 7) * 128 + 32 * lr;
#pragma unroll
            for (int i = 0; i < 4; ++i) {
                *(f32x4*)(dst + 8 * i) = f32x4{acc[0][i], acc[1][i], acc[2][i], acc[3][i]};
                *(f32x4*)(dst + 8 * i + 4) = f32x4{acc[4][i], acc[5][i], acc[6][i], acc[7][i]};
            }
            if (lr == 0) { wml[(w * 8 + (lane & 7)) * 2] = m_run; wml[(w * 8 + (lane & 7)) * 2 + 1] = l_run; }
        }
    }
    __syncthreads();
    {
        float M = -INFINITY;
#pragma unroll
        for (int ww = 1; ww < 8; ++ww) M = fmaxf(M, wml[(ww * 8 + w) * 2]);
        float L = 0.f, o0 = 0.f, o1 = 0.f;
#pragma unroll
        for (int ww = 1; ww < 8; ++ww) {
            const float f = ex2(wml[(ww * 8 + w) * 2] - M);
            L = fmaf(f, wml[(ww * 8 + w) * 2 + 1], L);
            const float2 v = *(const float2*)(lds + (ww - 1) * 4096 + w * 128 + 2 * lane);
            o0 = fmaf(f, v.x, o0); o1 = fmaf(f, v.y, o1);
        }
        v4u u = {__float_as_uint(o0), tag, __float_as_uint(o1), tag};
        __builtin_amdgcn_raw_buffer_store_b128(u, rs, (int)(WS_P + ((b * 8 + w) * 64 + lane) * 16), 0, AUX_SC1);
        if (lane == 0) { v4u ml = {__float_as_uint(M), tag, __float_as_uint(L), tag};
            __builtin_amdgcn_raw_buffer_store_b128(ml, rs, (int)(WS_ML + (b * 8 + w) * 16), 0, AUX_SC1); }
    }
    STAMP(6);
    if (W_MODE == 0 && w != 0) ISSUE_W();
    const int hq_l = rr >> 3, d0p = (rr & 7) * 8;
    const int src = kvh * 64 + (tid >> 3);
    const unsigned char* pp = ws + WS_P + ((src * 8 + hq_l) * 64 + d0p + (tid & 7)) * 16;
    const unsigned char* mp = ws + WS_ML + (src * 8 + hq_l) * 16;
    v4u pv, ml;
    asm volatile("global_load_dwordx4 %0, %2, off sc1\n\tglobal_load_dwordx4 %1, %3, off sc1" : "=&v"(pv), "=&v"(ml) : "v"(pp), "v"(mp) : "memory");
    asm volatile("s_waitcnt vmcnt(2)" ::: "memory");
    KV W;
    {
        const LAS v4u* wl = (const LAS v4u*)((LAS unsigned char*)lds + wslot) + lane;
#pragma unroll
        for (int i = 0; i < 8; ++i) W.r[i] = wl[64 * i];
    }
    const float g = gts[0], omg = gts[1];
    float a[8];
#pragma unroll
    for (int k = 0; k < 8; ++k) a[k] = 0.f;
#pragma unroll
    for (int i = 0; i < 4; ++i) {
        const float ch = hs[4 * i + lr];
#pragma unroll
        for (int k = 0; k < 4; ++k) { a[2 * k] = fmaf(ch, bflo(W.r[i][k]), a[2 * k]); a[2 * k + 1] = fmaf(ch, bfhi(W.r[i][k]), a[2 * k + 1]); }
    }
    STAMP(7);
    {
        asm volatile("s_waitcnt vmcnt(0)" : "+v"(pv), "+v"(ml));
        u32 spins = 0;
        while (!__all(tagok(pv, tag) && tagok(ml, tag))) {
            __builtin_amdgcn_s_sleep(1);
            poll16x2(pp, mp, pv, ml);
            if (++spins > SPIN_CAP) break;
        }
        const float ms = __uint_as_float(ml[0]), ls = __uint_as_float(ml[2]);
        float Mw = fmaxf(ms, dpp<ROR8>(ms)); Mw = xrow_max(Mw);
        const float e = ex2(ms - Mw);
        float L = e * ls, o0 = e * __uint_as_float(pv[0]), o1 = e * __uint_as_float(pv[2]);
        L += dpp<ROR8>(L); o0 += dpp<ROR8>(o0); o1 += dpp<ROR8>(o1);
        L = xrow_sum(L); o0 = xrow_sum(o0); o1 = xrow_sum(o1);
        if (lane < 8) *(float2*)(cw + w * 16 + 2 * lane) = make_float2(o0, o1);
        if (lane == 0) { cml[2 * w] = Mw; cml[2 * w + 1] = L; }
    }
    __syncthreads();
    STAMP(8);
    {
        float M = -INFINITY;
#pragma unroll
        for (int ww = 0; ww < 8; ++ww) M = fmaxf(M, cml[2 * ww]);
        float L = 0.f, o = 0.f;
#pragma unroll
        for (int ww = 0; ww < 8; ++ww) { const float f = ex2(cml[2 * ww] - M); L = fmaf(f, cml[2 * ww + 1], L); o = fmaf(f, cw[ww * 16 + lc], o); }
        const float ov = rbf(o / L) * g;
#pragma unroll
        for (int i = 0; i < 4; ++i) {
            const float co = __shfl(ov, 4 * i + lr);
#pragma unroll
            for (int k = 0; k < 4; ++k) { a[2 * k] = fmaf(co, bflo(W.r[4 + i][k]), a[2 * k]); a[2 * k + 1] = fmaf(co, bfhi(W.r[4 + i][k]), a[2 * k + 1]); }
        }
#pragma unroll
        for (int k = 0; k < 8; ++k) a[k] = xrow_sum(a[k]);
        const float v0 = lr == 0 ? a[0] : lr == 1 ? a[2] : lr == 2 ? a[4] : a[6];
        const float v1 = lr == 0 ? a[1] : lr == 1 ? a[3] : lr == 2 ? a[5] : a[7];
        v4u u = {__float_as_uint(v0), tag, __float_as_uint(v1), tag};
        __builtin_amdgcn_raw_buffer_store_b128(u, rs, (int)(WS_OUTP + (b * 512 + (ocol >> 1) + lr) * 16), 0, AUX_SC1);
        STAMP(9); STAMP(10);
        if (b < 64) {
            const unsigned char* fp = ws + WS_OUTP + ((tid >> 3) * 512 + 8 * b + (tid & 7)) * 16;
            const u32 xo = x[16 * b + (tid & 15)];
            v4u f0, f1, f2, f3;
            u32 spins = 0;
            for (;;) {
                asm volatile("global_load_dwordx4 %0, %4, off sc1\n\tglobal_load_dwordx4 %1, %5, off sc1\n\tglobal_load_dwordx4 %2, %6, off sc1\n\tglobal_load_dwordx4 %3, %7, off sc1\n\ts_waitcnt vmcnt(0)"
                             : "=&v"(f0), "=&v"(f1), "=&v"(f2), "=&v"(f3) : "v"(fp), "v"(fp + 64 * 8192), "v"(fp + 128 * 8192), "v"(fp + 192 * 8192) : "memory");
                const bool ok = tagok(f0, tag) && tagok(f1, tag) && tagok(f2, tag) && tagok(f3, tag);
                if (__all(ok) || ++spins > SPIN_CAP) break;
                __builtin_amdgcn_s_sleep(1);
            }
            STAMP(10);
            float s0 = (__uint_as_float(f0[0]) + __uint_as_float(f1[0])) + (__uint_as_float(f2[0]) + __uint_as_float(f3[0]));
            float s1 = (__uint_as_float(f0[2]) + __uint_as_float(f1[2])) + (__uint_as_float(f2[2]) + __uint_as_float(f3[2]));
            s0 += dpp<ROR8>(s0); s1 += dpp<ROR8>(s1); s0 = xrow_sum(s0); s1 = xrow_sum(s1);
            if (lane < 8) *(float2*)(fin + w * 16 + 2 * lane) = make_float2(s0, s1);
            __syncthreads();
            if (tid < 16) {
                float sum = 0.f;
#pragma unroll
                for (int ww = 0; ww < 8; ++ww) sum += fin[ww * 16 + tid];
                out[16 * b + tid] = (u16)cvt1(sum + omg * bf2f(xo));
            }
            if (b == 0 && tid == 0) __hip_atomic_store((GAS u32*)(ws + WS_EPOCH), tag, __ATOMIC_RELAXED, __HIP_MEMORY_SCOPE_AGENT);
        }
    }
    }
    STAMP(19);
}

extern "C" void kernel_launch(void* const* d_in, const int* in_sizes, int n_in, void* d_out, int out_size, void* d_ws, size_t ws_size, hipStream_t stream) {
    const u16* x = (const u16*)d_in[0]; const u16* kc = (const u16*)d_in[1]; const u16* vc = (const u16*)d_in[2];
    const u16* wq = (const u16*)d_in[3]; const u16* wo = (const u16*)d_in[4];
    const u16* wr = (const u16*)d_in[6]; const u16* wg = (const u16*)d_in[7]; const u16* wu = (const u16*)d_in[8]; const u16* wd = (const u16*)d_in[9]; const u16* gw = (const u16*)d_in[10];
    fused_decode<<<NWG, NTHR, 0, stream>>>(x, kc, vc, wq, wo, wr, wg, wu, wd, gw, (u16*)d_out, (unsigned char*)d_ws);
}
```

```cpp
#include <hip/hip_runtime.h>
#include <hip/hip_bf16.h>
#include <cstdint>

typedef unsigned short u16;
typedef unsigned int u32;
typedef unsigned long long u64;
typedef u32 v4u __attribute__((ext_vector_type(4)));
typedef u32 v2u __attribute__((ext_vector_type(2)));
typedef float f32x4 __attribute__((ext_vector_type(4)));
typedef __bf16 bf16x8 __attribute__((ext_vector_type(8)));
#define GAS __attribute__((address_space(1)))
#define LAS __attribute__((address_space(3)))

#define ROUNDS_F 6, 6, 4, 6, 5, 3, 2, 0
#define ROUNDS_N 6, 6, 4, 6, 5, 3, 2, 0
#define NT_F_ 32
#define NT_N_ 32

namespace {
constexpr int D = 1024, SK = 65536, DH = 128, FF = 3584;
constexpr int NWG = 256, NTHR = 512;
constexpr u32 OOB = 0x40000000u;
constexpr u32 NT_F = NT_F_, NT_N = NT_N_;

constexpr u32 WS_EPOCH = 0;
constexpr u32 WS_QG = 4096;
constexpr u32 WS_QP = WS_QG + 4 * 256 * 16;
constexpr u32 WS_GP = WS_QP + 256 * 64 * 16;
constexpr u32 WS_UP = WS_GP + 256 * 64 * 16;
constexpr u32 WS_ML = WS_UP + 256 * 64 * 16;
constexpr u32 WS_P = WS_ML + 256 * 8 * 16;
constexpr u32 WS_OUTP = WS_P + 256 * 8 * 64 * 16;
constexpr u32 WS_END = WS_OUTP + 256 * 512 * 16;
constexpr int AUX_SC1 = 16;
constexpr u32 SPIN_CAP = 1u << 18;

#define STAMP(k) do { } while (0)
#define STAMPW(k, wv_) do { } while (0)

__device__ __forceinline__ float bflo(u32 w) { return __builtin_bit_cast(float, w << 16); }
__device__ __forceinline__ float bfhi(u32 w) { return __builtin_bit_cast(float, w & 0xffff0000u); }
__device__ __forceinline__ float bf2f(u32 b) { return __builtin_bit_cast(float, b << 16); }
__device__ __forceinline__ u32 cvt1(float v) { return (u32)__builtin_bit_cast(u16, (__bf16)v); }
__device__ __forceinline__ float rbf(float f) { return bf2f(cvt1(f)); }
template <int CTRL> __device__ __forceinline__ float dpp(float x) {
    return __builtin_bit_cast(float, __builtin_amdgcn_mov_dpp(__builtin_bit_cast(int, x), CTRL, 0xf, 0xf, true));
}
template <int CTRL> __device__ __forceinline__ u32 dppu(u32 x) { return (u32)__builtin_amdgcn_mov_dpp((int)x, CTRL, 0xf, 0xf, true); }
constexpr int ROR1 = 0x121, ROR2 = 0x122, ROR4 = 0x124, ROR8 = 0x128, QXOR1 = 0xB1;
__device__ __forceinline__ float xrow_sum(float x) {
    auto s = __builtin_amdgcn_permlane16_swap(__float_as_uint(x), __float_as_uint(x), false, false);
    x = __uint_as_float(s[0]) + __uint_as_float(s[1]);
    auto t = __builtin_amdgcn_permlane32_swap(__float_as_uint(x), __float_as_uint(x), false, false);
    return __uint_as_float(t[0]) + __uint_as_float(t[1]);
}
__device__ __forceinline__ float xrow_max(float x) {
    auto s = __builtin_amdgcn_permlane16_swap(__float_as_uint(x), __float_as_uint(x), false, false);
    x = fmaxf(__uint_as_float(s[0]), __uint_as_float(s[1]));
    auto t = __builtin_amdgcn_permlane32_swap(__float_as_uint(x), __float_as_uint(x), false, false);
    return fmaxf(__uint_as_float(t[0]), __uint_as_float(t[1]));
}
__device__ __forceinline__ float rs32(float x, float y) {
    auto t = __builtin_amdgcn_permlane32_swap(__float_as_uint(x), __float_as_uint(y), false, false);
    return __uint_as_float(t[0]) + __uint_as_float(t[1]);
}
__device__ __forceinline__ float rs16(float x, float y) {
    auto t = __builtin_amdgcn_permlane16_swap(__float_as_uint(x), __float_as_uint(y), false, false);
    return __uint_as_float(t[0]) + __uint_as_float(t[1]);
}
__device__ __forceinline__ f32x4 mfma(v4u a, v4u b, f32x4 c) {
    return __builtin_amdgcn_mfma_f32_16x16x32_bf16(__builtin_bit_cast(bf16x8, a), __builtin_bit_cast(bf16x8, b), c, 0, 0, 0);
}
__device__ __forceinline__ v4u ldnt(const void* p) { return __builtin_nontemporal_load((const GAS v4u*)p); }
__device__ __forceinline__ float ex2(float x) { return __builtin_amdgcn_exp2f(x); }
__device__ __forceinline__ float sigmoidf_(float z) { return 1.f / (1.f + __expf(-z)); }
__device__ __forceinline__ int fresh_lane() {
    int l = __builtin_amdgcn_mbcnt_hi(~0u, __builtin_amdgcn_mbcnt_lo(~0u, 0u));
    asm volatile("" : "+v"(l));
    return l;
}
#define SB() __builtin_amdgcn_sched_barrier(0)
__device__ __forceinline__ v4u poll16(const unsigned char* p) {
    v4u r;
    asm volatile("global_load_dwordx4 %0, %1, off sc1\n\ts_waitcnt vmcnt(0)" : "=&v"(r) : "v"(p) : "memory");
    return r;
}
__device__ __forceinline__ void poll16x2(const unsigned char* p, const unsigned char* q, v4u& a, v4u& b) {
    asm volatile("global_load_dwordx4 %0, %2, off sc1\n\tglobal_load_dwordx4 %1, %3, off sc1\n\ts_waitcnt vmcnt(0)" : "=&v"(a), "=&v"(b) : "v"(p), "v"(q) : "memory");
}
__device__ __forceinline__ bool tagok(const v4u& u, u32 tag) { return u[1] == tag && u[3] == tag; }

struct KV { v4u r[8]; };
__device__ __forceinline__ void gemv_block(const v4u* wv, const float (&xv)[4], float (&a)[8]) {
#pragma unroll
    for (int k = 0; k < 8; ++k) a[k] = 0.f;
#pragma unroll
    for (int i = 0; i < 4; ++i)
#pragma unroll
        for (int k = 0; k < 4; ++k) { a[2 * k] = fmaf(xv[i], bflo(wv[i][k]), a[2 * k]); a[2 * k + 1] = fmaf(xv[i], bfhi(wv[i][k]), a[2 * k + 1]); }
#pragma unroll
    for (int k = 0; k < 8; ++k) a[k] = xrow_sum(a[k]);
}
__device__ __forceinline__ u32 tile_off(int n, int w, bool ffn, u32 tbase) {
    constexpr int mf[8] = {ROUNDS_F}, mn[8] = {ROUNDS_N};
    int start = 0, m = 0;
#pragma unroll
    for (int r = 0; r < 8; ++r) { const int mr = ffn ? mf[r] : mn[r]; if (r < n) start += mr; if (r == n) m = mr; }
    return (n < 8 && w <= m) ? (tbase + (u32)(start + w - 1)) * 8192u : OOB;
}
}

#define LD4(R0, R1, R2, R3, VO, RS, O1, O2, O3) asm volatile( \
        "buffer_load_dwordx4 %0, %4, %5, 0 offen nt\n\tbuffer_load_dwordx4 %1, %4, %5, 0 offen offset:" #O1 " nt\n\t" \
        "buffer_load_dwordx4 %2, %4, %5, 0 offen offset:" #O2 " nt\n\tbuffer_load_dwordx4 %3, %4, %5, 0 offen offset:" #O3 " nt" \
        : "=&v"(R0), "=&v"(R1), "=&v"(R2), "=&v"(R3) : "v"(VO), "s"(RS))
#define LD4S(R0, R1, R2, R3, VO, RS, S1, S2, S3) asm volatile( \
        "buffer_load_dwordx4 %0, %4, %5, 0 offen\n\tbuffer_load_dwordx4 %1, %4, %5, %6 offen\n\t" \
        "buffer_load_dwordx4 %2, %4, %5, %7 offen\n\tbuffer_load_dwordx4 %3, %4, %5, %8 offen" \
        : "=&v"(R0), "=&v"(R1), "=&v"(R2), "=&v"(R3) : "v"(VO), "s"(RS), "s"(S1), "s"(S2), "s"(S3))
#define LDK4(BUF, H, TO) LD4(BUF.r[4 * (H)], BUF.r[4 * (H) + 1], BUF.r[4 * (H) + 2], BUF.r[4 * (H) + 3], kvo + (TO) + 4096u * (H), rk, 128, 2048, 2176)
#define LDV4(BUF, H, TO) LD4(BUF.r[4 * (H)], BUF.r[4 * (H) + 1], BUF.r[4 * (H) + 2], BUF.r[4 * (H) + 3], vvo + (TO) + 4096u * (H), rv, 1024, 2048, 3072)
#define ISSUE_K(BUF, TO) do { LDK4(BUF, 0, TO); LDK4(BUF, 1, TO); } while (0)
#define ISSUE_V(BUF, TO) do { LDV4(BUF, 0, TO); LDV4(BUF, 1, TO); } while (0)
#define DMA1(LDSA, VO, RS, SO) asm volatile("s_mov_b32 m0, %0\n\ts_nop 0\n\tbuffer_load_dwordx4 %1, %2, %3 offen nt lds" :: "s"(LDSA), "v"(VO), "s"(RS), "s"(SO) : "m0", "memory")
#define DMA_K(LDSA, TO) do { _Pragma("unroll") for (int i_ = 0; i_ < 8; ++i_) DMA1((LDSA) + 1024u * i_, kvo + (TO), rk, (u32)((i_ >> 1) * 2048 + (i_ & 1) * 128)); } while (0)
#define DMA_V(LDSA, TO) do { _Pragma("unroll") for (int i_ = 0; i_ < 8; ++i_) DMA1((LDSA) + 8192u + 1024u * i_, vvo + (TO), rv, (u32)(i_ * 1024)); } while (0)
#define ISSUE_W() do { const int l_ = fresh_lane(); \
                       const u32 woo_ = (u32)(((16 * b + (l_ >> 4)) * D + 8 * (l_ & 15)) * 2); \
                       const u32 wdo_ = has_ffn ? (u32)(((128 * cgf + 16 * ks + (l_ >> 4)) * D + 8 * (l_ & 15)) * 2) : OOB; \
                       for (int c_ = 0; c_ < 8; ++c_) { \
                       _Pragma("unroll") for (int i_ = 0; i_ < 4; ++i_) DMA1(ldsbase + WBASE + 8192u * c_ + 1024u * i_, wdo_, rd, (u32)(8192 * i_ + 256 * c_)); \
                       _Pragma("unroll") for (int i_ = 0; i_ < 4; ++i_) DMA1(ldsbase + WBASE + 8192u * c_ + 4096u + 1024u * i_, woo_, ro, (u32)(8192 * i_ + 256 * c_)); } } while (0)
#define WAIT8(BUF, N) asm volatile("s_waitcnt vmcnt(%8)" : "+v"(BUF.r[0]), "+v"(BUF.r[1]), "+v"(BUF.r[2]), "+v"(BUF.r[3]), "+v"(BUF.r[4]), "+v"(BUF.r[5]), "+v"(BUF.r[6]), "+v"(BUF.r[7]) : "n"(N))

__global__ void __launch_bounds__(NTHR, 2)
fused_decode(const u16* __restrict__ x, const u16* __restrict__ kc, const u16* __restrict__ vc, const u16* __restrict__ wq, const u16* __restrict__ wo,
             const u16* __restrict__ wr, const u16* __restrict__ wg, const u16* __restrict__ wu, const u16* __restrict__ wd, const u16* __restrict__ gw,
             u16* __restrict__ out, unsigned char* ws)
{
    const int tid = threadIdx.x, lane = tid & 63, w = __builtin_amdgcn_readfirstlane(tid >> 6);
    __shared__ __attribute__((aligned(16))) float lds[4 * 4096 + 16384 + 3 * 1024 + 512 + 512 + 2048];
    float* gpart = lds + 4 * 4096 + 16384;
    float* upart = gpart + 1024;
    float* qpart = gpart + 2048;
    float* sm = gpart + 3072;
    u32* qs = (u32*)(gpart + 3072 + 512);
    constexpr u32 WBASE = 4 * 16384;
    constexpr int REC56 = 4 * 4096 + 16384 + 3 * 1024 + 1024;
    float* gsc = sm;
    float* wml = sm + 32;
    float* cw = sm + 160;
    float* cml = sm + 288;
    float* fin = sm + 320;
    volatile LAS u32* qflag = (volatile LAS u32*)(sm + 480);
    volatile LAS u32* gucnt = (volatile LAS u32*)(sm + 481);
    float* gts = sm + 484;
    float* hs = sm + 488;

    const int b = blockIdx.x, kvh = b >> 6, rr = b & 63, G = b >> 3, ks = b & 7;
    const __amdgpu_buffer_rsrc_t rs = __builtin_amdgcn_make_buffer_rsrc((void*)ws, 0, (int)WS_END, 0x00020000);
    const bool has_ffn = (G & 7) != 7;
    const int cgf = kvh * 7 + (G & 7);
    const int lc = lane & 15, lr = lane >> 4;
    STAMP(0);

    const int grow = 128 * ks + 16 * w + lr;
    u32 E, xr[4], xp, gp, rp;
    asm volatile("global_load_dword %0, %8, %9 sc1\n\t"
                 "global_load_ushort %1, %10, %11\n\tglobal_load_ushort %2, %10, %11 offset:8\n\tglobal_load_ushort %3, %10, %11 offset:16\n\tglobal_load_ushort %4, %10, %11 offset:24\n\t"
                 "global_load_dword %5, %12, %11\n\tglobal_load_dword %6, %12, %13\n\tglobal_load_dword %7, %12, %14"
                 : "=&v"(E), "=&v"(xr[0]), "=&v"(xr[1]), "=&v"(xr[2]), "=&v"(xr[3]), "=&v"(xp), "=&v"(gp), "=&v"(rp)
                 : "v"(0u), "s"(ws), "v"((u32)(grow * 2)), "s"(x), "v"((u32)(tid * 4)), "s"(gw), "s"(wr));
    if (tid == 0) { *qflag = 0u; *gucnt = 0u; }
    SB();
    const __amdgpu_buffer_rsrc_t rq = __builtin_amdgcn_make_buffer_rsrc((void*)wq, 0, D * 4096 * 2, 0x00020000);
    const __amdgpu_buffer_rsrc_t rk = __builtin_amdgcn_make_buffer_rsrc((void*)(kc + (size_t)kvh * SK * DH), 0, SK * DH * 2, 0x00020000);
    const __amdgpu_buffer_rsrc_t rv = __builtin_amdgcn_make_buffer_rsrc((void*)(vc + (size_t)kvh * SK * DH), 0, SK * DH * 2, 0x00020000);
    const __amdgpu_buffer_rsrc_t rg = __builtin_amdgcn_make_buffer_rsrc((void*)wg, 0, D * FF * 2, 0x00020000);
    const __amdgpu_buffer_rsrc_t ru = __builtin_amdgcn_make_buffer_rsrc((void*)wu, 0, D * FF * 2, 0x00020000);
    const __amdgpu_buffer_rsrc_t rd = __builtin_amdgcn_make_buffer_rsrc((void*)wd, 0, FF * D * 2, 0x00020000);
    const __amdgpu_buffer_rsrc_t ro = __builtin_amdgcn_make_buffer_rsrc((void*)wo, 0, 4096 * D * 2, 0x00020000);
    const u32 kvo = (u32)((4 * ((lc >> 1) & 1) + (lc >> 2)) * 256 + (lc & 1) * 64 + lr * 16);
    const u32 vvo = (u32)(lane * 16);
    const u32 tbase = has_ffn ? NT_F * (u32)rr : 56u * NT_F + NT_N * (u32)(rr - 56);
    KV KA, VA, KB, VB;
    const u32 ldsbase = (u32)(size_t)(LAS void*)lds;

    KV WQ;
    LD4S(WQ.r[0], WQ.r[1], WQ.r[2], WQ.r[3], (u32)((grow * 4096 + 128 * G + 8 * lc) * 2), rq, 4 * 8192, 8 * 8192, 12 * 8192);
    SB();
    __builtin_amdgcn_s_barrier();
    SB();
    const u32 guo = has_ffn ? (u32)((grow * FF + 128 * cgf + 8 * lc) * 2) : OOB;
    {
        const u32 gu0 = w == 0 ? OOB : guo;
        LD4S(KB.r[0], KB.r[1], KB.r[2], KB.r[3], gu0, rg, 4 * FF * 2, 8 * FF * 2, 12 * FF * 2);
        LD4S(KB.r[4], KB.r[5], KB.r[6], KB.r[7], gu0, ru, 4 * FF * 2, 8 * FF * 2, 12 * FF * 2);
    }
    asm volatile("s_waitcnt vmcnt(8)" : "+v"(WQ.r[0]), "+v"(WQ.r[1]), "+v"(WQ.r[2]), "+v"(WQ.r[3]),
                 "+v"(E), "+v"(xr[0]), "+v"(xr[1]), "+v"(xr[2]), "+v"(xr[3]), "+v"(xp), "+v"(gp), "+v"(rp));
    float xv[4];
#pragma unroll
    for (int i = 0; i < 4; ++i) xv[i] = bf2f(xr[i]);
    {
        float a[8];
        gemv_block(&WQ.r[0], xv, a);
        if (lr == 0) { float* dst = qpart + w * 128 + 8 * lc; *(f32x4*)dst = f32x4{a[0], a[1], a[2], a[3]}; *(f32x4*)(dst + 4) = f32x4{a[4], a[5], a[6], a[7]}; }
        float z = rbf(bflo(xp) * bflo(gp)) + rbf(bfhi(xp) * bfhi(gp));
        float r = bflo(xp) * bflo(rp) + bfhi(xp) * bfhi(rp);
        z += dpp<ROR1>(z); z += dpp<ROR2>(z); z += dpp<ROR4>(z); z += dpp<ROR8>(z); z = xrow_sum(z);
        r += dpp<ROR1>(r); r += dpp<ROR2>(r); r += dpp<ROR4>(r); r += dpp<ROR8>(r); r = xrow_sum(r);
        if (lane == 0) { gsc[2 * w] = z; gsc[2 * w + 1] = r; }
    }
    SB();
    __syncthreads();
    STAMP(1);
    const u32 tag = __builtin_amdgcn_readfirstlane(E) + 1u;
    auto gate_up = [&]() {
        if (has_ffn) {
            float ag[8], au[8];
            gemv_block(&KB.r[0], xv, ag);
            gemv_block(&KB.r[4], xv, au);
            if (lr == 0) {
                float* dg = gpart + w * 128 + 8 * lc; float* du = upart + w * 128 + 8 * lc;
                *(f32x4*)dg = f32x4{ag[0], ag[1], ag[2], ag[3]}; *(f32x4*)(dg + 4) = f32x4{ag[4], ag[5], ag[6], ag[7]};
                *(f32x4*)du = f32x4{au[0], au[1], au[2], au[3]}; *(f32x4*)(du + 4) = f32x4{au[4], au[5], au[6], au[7]};
            }
            asm volatile("s_waitcnt lgkmcnt(0)" ::: "memory");
            u32 old = 0;
            if (lane == 0) old = __hip_atomic_fetch_add((LAS u32*)gucnt, 1u, __ATOMIC_RELAXED, __HIP_MEMORY_SCOPE_WORKGROUP);
            if (__builtin_amdgcn_readfirstlane(old) == 7u) {
                float g0 = 0.f, g1 = 0.f, u0 = 0.f, u1 = 0.f;
#pragma unroll
                for (int ww = 0; ww < 8; ++ww) { const float2 vg = *(const float2*)(gpart + ww * 128 + 2 * lane), vu = *(const float2*)(upart + ww * 128 + 2 * lane); g0 += vg.x; g1 += vg.y; u0 += vu.x; u1 += vu.y; }
                v4u ug = {__float_as_uint(g0), tag, __float_as_uint(g1), tag}, uu_ = {__float_as_uint(u0), tag, __float_as_uint(u1), tag};
                __builtin_amdgcn_raw_buffer_store_b128(ug, rs, (int)(WS_GP + (b * 64 + lane) * 16), 0, AUX_SC1);
                __builtin_amdgcn_raw_buffer_store_b128(uu_, rs, (int)(WS_UP + (b * 64 + lane) * 16), 0, AUX_SC1);
            }
        }
    };

    if (w == 0)
        {
            float s0 = 0.f, s1 = 0.f;
#pragma unroll
            for (int ww = 0; ww < 8; ++ww) { const float2 v = *(const float2*)(qpart + ww * 128 + 2 * lane); s0 += v.x; s1 += v.y; }
            v4u u = {__float_as_uint(s0), tag, __float_as_uint(s1), tag};
            __builtin_amdgcn_raw_buffer_store_b128(u, rs, (int)(WS_QP + (b * 64 + lane) * 16), 0, AUX_SC1);
        }
    WAIT8(KB, 0);
    if (w != 0) gate_up();
    STAMPW(2, SW);

    constexpr float C2 = 0.08838834764831845f * 1.4426950408889634f;
    const bool uhi_ = (lane >> 3) & 1;
    const u32 psh = uhi_ ? 16u : 0u;
    float m_run = -INFINITY, l_run = 0.f;
    f32x4 acc[8];
#pragma unroll
    for (int d = 0; d < 8; ++d) acc[d] = f32x4{0.f, 0.f, 0.f, 0.f};
    v4u qf[2];
    float sc[4];
    auto qk = [&](const KV& Kb) {
#pragma unroll
        for (int t = 0; t < 4; ++t) {
            f32x4 c = {0.f, 0.f, 0.f, 0.f};
            c = mfma(Kb.r[2 * t], qf[0], c);
            c = mfma(Kb.r[2 * t + 1], qf[1], c);
            const float t0 = c[0] + dpp<ROR8>(c[1]);
            const float t1 = c[3] + dpp<ROR8>(c[2]);
            sc[t] = uhi_ ? t1 : t0;
        }
    };
    auto softmax_pv = [&](const KV& Vb) {
        float mx = fmaxf(fmaxf(sc[0], sc[1]), fmaxf(sc[2], sc[3]));
        mx = fmaxf(mx, dpp<ROR8>(mx));
        mx = xrow_max(mx);
        const float mn = fmaxf(m_run, mx * C2);
        const float alpha = ex2(m_run - mn);
        m_run = mn;
        v4u pf;
        float ps = 0.f;
#pragma unroll
        for (int t = 0; t < 4; ++t) { const float p = ex2(fmaf(sc[t], C2, -mn)); const u32 pb = cvt1(p); ps += bf2f(pb); pf[t] = pb << psh; }
        l_run = fmaf(l_run, alpha, ps);
#pragma unroll
        for (int d = 0; d < 8; ++d) acc[d] *= alpha;
#pragma unroll
        for (int d = 0; d < 8; ++d) {
            v4u a;
#pragma unroll
            for (int q = 0; q < 4; ++q) a[q] = __builtin_amdgcn_perm(Vb.r[2 * q + 1][d >> 1], Vb.r[2 * q][d >> 1], (d & 1) ? 0x07060302u : 0x05040100u);
            acc[d] = mfma(a, pf, acc[d]);
        }
    };

    if (w == 0) {
        const int uo = ((G * 8 + (lane >> 3)) * 64 + ks * 8 + (lane & 7)) * 16;
        v4u qa = poll16(ws + WS_QP + uo);
        u32 spins = 0;
        while (!tagok(qa, tag) && ++spins < SPIN_CAP) qa = poll16(ws + WS_QP + uo);
        STAMP(11);
        float s0 = __uint_as_float(qa[0]), s1 = __uint_as_float(qa[2]);
        s0 += dpp<ROR8>(s0); s1 += dpp<ROR8>(s1); s0 = xrow_sum(s0); s1 = xrow_sum(s1);
        const u32 pr = cvt1(s0) | (cvt1(s1) << 16);
        const u32 nb = dppu<QXOR1>(pr);
        if (lane < 8 && (lane & 1) == 0) {
            v4u uq = {pr, tag, nb, tag};
            __builtin_amdgcn_raw_buffer_store_b128(uq, rs, (int)(WS_QG + (kvh * 256 + rr * 4 + (lane >> 1)) * 16), 0, AUX_SC1);
        }
        const unsigned char* qp = ws + WS_QG + (kvh * 256 + lane) * 16;
        v4u q0, q1u, q2u, q3u;
        spins = 0;
        do {
            asm volatile("global_load_dwordx4 %0, %4, off sc1\n\tglobal_load_dwordx4 %1, %4, off offset:1024 sc1\n\tglobal_load_dwordx4 %2, %4, off offset:2048 sc1\n\tglobal_load_dwordx4 %3, %4, off offset:3072 sc1\n\ts_waitcnt vmcnt(0)"
                         : "=&v"(q0), "=&v"(q1u), "=&v"(q2u), "=&v"(q3u) : "v"(qp) : "memory");
        } while (!(tagok(q0, tag) && tagok(q1u, tag) && tagok(q2u, tag) && tagok(q3u, tag)) && ++spins < SPIN_CAP);
        *(v2u*)(qs + lane * 2) = v2u{q0[0], q0[2]};
        *(v2u*)(qs + (64 + lane) * 2) = v2u{q1u[0], q1u[2]};
        *(v2u*)(qs + (128 + lane) * 2) = v2u{q2u[0], q2u[2]};
        *(v2u*)(qs + (192 + lane) * 2) = v2u{q3u[0], q3u[2]};
        asm volatile("s_waitcnt lgkmcnt(0)" ::: "memory");
        *qflag = 1u;
        STAMP(12);
        LD4S(KB.r[0], KB.r[1], KB.r[2], KB.r[3], guo, rg, 4 * FF * 2, 8 * FF * 2, 12 * FF * 2);
        LD4S(KB.r[4], KB.r[5], KB.r[6], KB.r[7], guo, ru, 4 * FF * 2, 8 * FF * 2, 12 * FF * 2);
        WAIT8(KB, 0);
        gate_up();
        float z = 0.f, r = 0.f;
#pragma unroll
        for (int ww = 0; ww < 8; ++ww) { z += gsc[2 * ww]; r += gsc[2 * ww + 1]; }
        const float g = rbf(sigmoidf_(rbf(z))), sr = rbf(sigmoidf_(rbf(r)));
        const float omg = rbf(1.f - g);
        float h0 = 0.f, h1 = 0.f;
        if (has_ffn) {
            v4u gu, uu;
            spins = 0;
            for (;;) {
                poll16x2(ws + WS_GP + uo, ws + WS_UP + uo, gu, uu);
                if (__all(tagok(gu, tag) && tagok(uu, tag)) || ++spins > SPIN_CAP) break;
                __builtin_amdgcn_s_sleep(8);
            }
            float g0 = __uint_as_float(gu[0]), g1 = __uint_as_float(gu[2]), u0 = __uint_as_float(uu[0]), u1 = __uint_as_float(uu[2]);
            g0 += dpp<ROR8>(g0); g1 += dpp<ROR8>(g1); u0 += dpp<ROR8>(u0); u1 += dpp<ROR8>(u1);
            g0 = rbf(xrow_sum(g0)); g1 = rbf(xrow_sum(g1)); u0 = rbf(xrow_sum(u0)); u1 = rbf(xrow_sum(u1));
            h0 = rbf(rbf(g0 * sigmoidf_(g0)) * u0) * (omg * sr);
            h1 = rbf(rbf(g1 * sigmoidf_(g1)) * u1) * (omg * sr);
        }
        if (lane < 8) *(float2*)(hs + 2 * lane) = make_float2(h0, h1);
        if (lane == 0) { gts[0] = g; gts[1] = omg; }
    } else {
        const u32 t0 = tile_off(0, w, has_ffn, tbase), t1 = tile_off(1, w, has_ffn, tbase), t2 = tile_off(2, w, has_ffn, tbase);
      if (w == 7) {
        ISSUE_W();
        asm volatile("s_waitcnt vmcnt(0)" ::: "memory");
      } else {
        const u32 slot = w <= 4 ? ldsbase + (u32)(w - 1) * 16384u : 0x2F000u;
        ISSUE_K(KA, t0);
        ISSUE_V(VA, t0);
        ISSUE_K(KB, t1);
        ISSUE_V(VB, t1);
        DMA_K(slot, t2);
        DMA_V(slot, t2);
        {
            u32 spins = 0;
            while (*qflag == 0u) { __builtin_amdgcn_s_sleep(1); if (++spins > 64u * SPIN_CAP) break; }
            STAMPW(13, SW);
        }
#pragma unroll
        for (int s = 0; s < 2; ++s) qf[s] = *(const v4u*)(qs + (lane & 7) * 64 + 32 * s + 16 * ((lane >> 3) & 1) + 4 * lr);
        u32 ta = tile_off(3, w, has_ffn, tbase);
        WAIT8(KA, 40); qk(KA); SB();
        ISSUE_K(KA, ta);
        WAIT8(VA, 40); softmax_pv(VA); SB();
        ISSUE_V(VA, ta);
        STAMPW(3, SW);
        WAIT8(KB, 40); qk(KB); SB();
        WAIT8(VB, 32); softmax_pv(VB); SB();
        STAMPW(4, SW);
        u32 tb = tile_off(4, w, has_ffn, tbase);
        asm volatile("s_waitcnt vmcnt(16)" ::: "memory");
        STAMPW(15, SW);
        {
            const bool vc_ = t2 != OOB;
            const LAS v4u* sl = (const LAS v4u*)(lds + (w - 1) * 4096) + lane;
            if (vc_) {
#pragma unroll
                for (int i = 0; i < 8; ++i) { KB.r[i] = sl[64 * i]; VB.r[i] = sl[512 + 64 * i]; }
                qk(KB);
            }
            SB();
            ISSUE_K(KB, tb);
            if (vc_) softmax_pv(VB);
            SB();
            ISSUE_V(VB, tb);
        }
        for (int n = 5; ta != OOB; n += 2) {
            WAIT8(KA, 24); qk(KA); SB();
            ta = tile_off(n, w, has_ffn, tbase);
            ISSUE_K(KA, ta);
            WAIT8(VA, 24); softmax_pv(VA); SB();
            ISSUE_V(VA, ta);
            const bool vb = tb != OOB;
            WAIT8(KB, 24);
            if (vb) qk(KB);
            SB();
            tb = tile_off(n + 1, w, has_ffn, tbase);
            ISSUE_K(KB, tb);
            WAIT8(VB, 24);
            if (vb) softmax_pv(VB);
            SB();
            ISSUE_V(VB, tb);
        }
        WAIT8(KA, 0); WAIT8(VA, 0); WAIT8(KB, 0); WAIT8(VB, 0);
      }
    }
    STAMP(5);
    STAMPW(14, 7); STAMPW(16, 1); STAMPW(17, 6);

    {
    const int lane = fresh_lane(), tid = w * 64 + lane, lc = lane & 15, lr = lane >> 4, ocol = 128 * w + 8 * lc;
    const bool uhi = (lane >> 3) & 1;
    {
        l_run += dpp<ROR8>(l_run); l_run = xrow_sum(l_run);
#pragma unroll
        for (int d = 0; d < 8; ++d)
#pragma unroll
            for (int i = 0; i < 4; ++i) acc[d][i] += dpp<ROR8>(acc[d][i]);
        if (!uhi && w != 0 && w != 7) {
            float* dst = lds + (w <= 4 ? (w - 1) * 4096 : REC56 + (w - 5) * 1024) + (lane & 7) * 128 + 32 * lr;
#pragma unroll
            for (int i = 0; i < 4; ++i) {
                *(f32x4*)(dst + 8 * i) = f32x4{acc[0][i], acc[1][i], acc[2][i], acc[3][i]};
                *(f32x4*)(dst + 8 * i + 4) = f32x4{acc[4][i], acc[5][i], acc[6][i], acc[7][i]};
            }
            if (lr == 0) { wml[(w * 8 + (lane & 7)) * 2] = m_run; wml[(w * 8 + (lane & 7)) * 2 + 1] = l_run; }
        }
    }
    __syncthreads();
    STAMP(18);
    {
        float M = -INFINITY;
#pragma unroll
        for (int ww = 1; ww < 7; ++ww) M = fmaxf(M, wml[(ww * 8 + w) * 2]);
        float L = 0.f, o0 = 0.f, o1 = 0.f;
#pragma unroll
        for (int ww = 1; ww < 7; ++ww) {
            const float f = ex2(wml[(ww * 8 + w) * 2] - M);
            L = fmaf(f, wml[(ww * 8 + w) * 2 + 1], L);
            const float2 v = *(const float2*)(lds + (ww <= 4 ? (ww - 1) * 4096 : REC56 + (ww - 5) * 1024) + w * 128 + 2 * lane);
            o0 = fmaf(f, v.x, o0); o1 = fmaf(f, v.y, o1);
        }
        v4u u = {__float_as_uint(o0), tag, __float_as_uint(o1), tag};
        __builtin_amdgcn_raw_buffer_store_b128(u, rs, (int)(WS_P + ((b * 8 + w) * 64 + lane) * 16), 0, AUX_SC1);
        if (lane == 0) { v4u ml = {__float_as_uint(M), tag, __float_as_uint(L), tag};
            __builtin_amdgcn_raw_buffer_store_b128(ml, rs, (int)(WS_ML + (b * 8 + w) * 16), 0, AUX_SC1); }
    }
    STAMP(6);
    const int hq_l = rr >> 3, d0p = (rr & 7) * 8;
    const int src = kvh * 64 + (tid >> 3);
    const unsigned char* pp = ws + WS_P + ((src * 8 + hq_l) * 64 + d0p + (tid & 7)) * 16;
    const unsigned char* mp = ws + WS_ML + (src * 8 + hq_l) * 16;
    v4u pv, ml;
    asm volatile("global_load_dwordx4 %0, %2, off sc1\n\tglobal_load_dwordx4 %1, %3, off sc1" : "=&v"(pv), "=&v"(ml) : "v"(pp), "v"(mp) : "memory");
    KV W;
    {
        const LAS v4u* wl = (const LAS v4u*)((LAS unsigned char*)lds + WBASE + 8192 * w) + lane;
#pragma unroll
        for (int i = 0; i < 8; ++i) W.r[i] = wl[64 * i];
    }
    const float g = gts[0], omg = gts[1];
    float a[8];
#pragma unroll
    for (int k = 0; k < 8; ++k) a[k] = 0.f;
#pragma unroll
    for (int i = 0; i < 4; ++i) {
        const float ch = hs[4 * i + lr];
#pragma unroll
        for (int k = 0; k < 4; ++k) { a[2 * k] = fmaf(ch, bflo(W.r[i][k]), a[2 * k]); a[2 * k + 1] = fmaf(ch, bfhi(W.r[i][k]), a[2 * k + 1]); }
    }
    float wf[4][8];
#pragma unroll
    for (int i = 0; i < 4; ++i)
#pragma unroll
        for (int k = 0; k < 4; ++k) { wf[i][2 * k] = bflo(W.r[4 + i][k]); wf[i][2 * k + 1] = bfhi(W.r[4 + i][k]); }
    STAMP(7);
    {
        asm volatile("s_waitcnt vmcnt(0)" : "+v"(pv), "+v"(ml));
        u32 spins = 0;
        while (!(tagok(pv, tag) && tagok(ml, tag)) && ++spins < SPIN_CAP) poll16x2(pp, mp, pv, ml);
        const float ms = __uint_as_float(ml[0]), ls = __uint_as_float(ml[2]);
        float Mw = fmaxf(ms, dpp<ROR8>(ms)); Mw = xrow_max(Mw);
        const float e = ex2(ms - Mw);
        float L = e * ls, o0 = e * __uint_as_float(pv[0]), o1 = e * __uint_as_float(pv[2]);
        L += dpp<ROR8>(L); o0 += dpp<ROR8>(o0); o1 += dpp<ROR8>(o1);
        L = xrow_sum(L); o0 = xrow_sum(o0); o1 = xrow_sum(o1);
        if (lane < 8) *(float2*)(cw + w * 16 + 2 * lane) = make_float2(o0, o1);
        if (lane == 0) { cml[2 * w] = Mw; cml[2 * w + 1] = L; }
    }
    __syncthreads();
    STAMP(8);
    {
        float M = -INFINITY;
#pragma unroll
        for (int ww = 0; ww < 8; ++ww) M = fmaxf(M, cml[2 * ww]);
        float L = 0.f, o = 0.f;
#pragma unroll
        for (int ww = 0; ww < 8; ++ww) { const float f = ex2(cml[2 * ww] - M); L = fmaf(f, cml[2 * ww + 1], L); o = fmaf(f, cw[ww * 16 + lc], o); }
        const float ov = rbf(o / L) * g;
#pragma unroll
        for (int i = 0; i < 4; ++i) {
            const float co = __shfl(ov, 4 * i + lr);
#pragma unroll
            for (int k = 0; k < 8; ++k) a[k] = fmaf(co, wf[i][k], a[k]);
        }
        const float b0 = rs32(a[0], a[4]), b1 = rs32(a[1], a[5]), b2 = rs32(a[2], a[6]), b3 = rs32(a[3], a[7]);
        const float v0 = rs16(b0, b2), v1 = rs16(b1, b3);
        v4u u = {__float_as_uint(v0), tag, __float_as_uint(v1), tag};
        __builtin_amdgcn_raw_buffer_store_b128(u, rs, (int)(WS_OUTP + (b * 512 + (ocol >> 1) + lr) * 16), 0, AUX_SC1);
        STAMP(9); STAMP(10);
        if (b < 64) {
            const unsigned char* fp = ws + WS_OUTP + ((tid >> 3) * 512 + 8 * b + (tid & 7)) * 16;
            const u32 xo = x[16 * b + (tid & 15)];
            v4u f0, f1, f2, f3;
            u32 spins = 0;
            do {
                asm volatile("global_load_dwordx4 %0, %4, off sc1\n\tglobal_load_dwordx4 %1, %5, off sc1\n\tglobal_load_dwordx4 %2, %6, off sc1\n\tglobal_load_dwordx4 %3, %7, off sc1\n\ts_waitcnt vmcnt(0)"
                             : "=&v"(f0), "=&v"(f1), "=&v"(f2), "=&v"(f3) : "v"(fp), "v"(fp + 64 * 8192), "v"(fp + 128 * 8192), "v"(fp + 192 * 8192) : "memory");
            } while (!(tagok(f0, tag) && tagok(f1, tag) && tagok(f2, tag) && tagok(f3, tag)) && ++spins < SPIN_CAP);
            STAMP(10);
            float s0 = (__uint_as_float(f0[0]) + __uint_as_float(f1[0])) + (__uint_as_float(f2[0]) + __uint_as_float(f3[0]));
            float s1 = (__uint_as_float(f0[2]) + __uint_as_float(f1[2])) + (__uint_as_float(f2[2]) + __uint_as_float(f3[2]));
            s0 += dpp<ROR8>(s0); s1 += dpp<ROR8>(s1); s0 = xrow_sum(s0); s1 = xrow_sum(s1);
            if (lane < 8) *(float2*)(fin + w * 16 + 2 * lane) = make_float2(s0, s1);
            __syncthreads();
            if (tid < 16) {
                float sum = 0.f;
#pragma unroll
                for (int ww = 0; ww < 8; ++ww) sum += fin[ww * 16 + tid];
                out[16 * b + tid] = (u16)cvt1(sum + omg * bf2f(xo));
            }
            if (b == 0 && tid == 0) __hip_atomic_store((GAS u32*)(ws + WS_EPOCH), tag, __ATOMIC_RELAXED, __HIP_MEMORY_SCOPE_AGENT);
        }
    }
    }
    STAMP(19);
}

extern "C" void kernel_launch(void* const* d_in, const int* in_sizes, int n_in, void* d_out, int out_size, void* d_ws, size_t ws_size, hipStream_t stream) {
    const u16* x = (const u16*)d_in[0]; const u16* kc = (const u16*)d_in[1]; const u16* vc = (const u16*)d_in[2];
    const u16* wq = (const u16*)d_in[3]; const u16* wo = (const u16*)d_in[4];
    const u16* wr = (const u16*)d_in[6]; const u16* wg = (const u16*)d_in[7]; const u16* wu = (const u16*)d_in[8]; const u16* wd = (const u16*)d_in[9]; const u16* gw = (const u16*)d_in[10];
    fused_decode<<<NWG, NTHR, 0, stream>>>(x, kc, vc, wq, wo, wr, wg, wu, wd, gw, (u16*)d_out, (unsigned char*)d_ws);
}
```

```cpp
#include <hip/hip_runtime.h>
#include <hip/hip_bf16.h>
#include <cstdint>

typedef unsigned short u16;
typedef unsigned int u32;
typedef unsigned long long u64;
typedef u32 v4u __attribute__((ext_vector_type(4)));
typedef u32 v2u __attribute__((ext_vector_type(2)));
typedef float f32x4 __attribute__((ext_vector_type(4)));
typedef __bf16 bf16x8 __attribute__((ext_vector_type(8)));
#define GAS __attribute__((address_space(1)))
#define LAS __attribute__((address_space(3)))

namespace {
constexpr int D = 1024, SK = 65536, DH = 128, FF = 3584;
constexpr int NWG = 256, NTHR = 512;
constexpr u32 OOB = 0x40000000u;

constexpr u32 WS_EPOCH = 0;
constexpr u32 WS_QG = 4096;
constexpr u32 WS_QP = WS_QG + 4 * 256 * 16;
constexpr u32 WS_GP = WS_QP + 256 * 64 * 16;
constexpr u32 WS_UP = WS_GP + 256 * 64 * 16;
constexpr u32 WS_ML = WS_UP + 256 * 64 * 16;
constexpr u32 WS_P = WS_ML + 256 * 8 * 16;
constexpr u32 WS_OUTP = WS_P + 256 * 8 * 64 * 16;
constexpr u32 WS_END = WS_OUTP + 256 * 512 * 16;
constexpr int AUX_SC1 = 16;
constexpr u32 SPIN_CAP = 1u << 18;

__device__ __forceinline__ float bflo(u32 w) { return __builtin_bit_cast(float, w << 16); }
__device__ __forceinline__ float bfhi(u32 w) { return __builtin_bit_cast(float, w & 0xffff0000u); }
__device__ __forceinline__ float bf2f(u32 b) { return __builtin_bit_cast(float, b << 16); }
__device__ __forceinline__ u32 cvt1(float v) { return (u32)__builtin_bit_cast(u16, (__bf16)v); }
__device__ __forceinline__ float rbf(float f) { return bf2f(cvt1(f)); }
template <int CTRL> __device__ __forceinline__ float dpp(float x) {
    return __builtin_bit_cast(float, __builtin_amdgcn_mov_dpp(__builtin_bit_cast(int, x), CTRL, 0xf, 0xf, true));
}
template <int CTRL> __device__ __forceinline__ u32 dppu(u32 x) { return (u32)__builtin_amdgcn_mov_dpp((int)x, CTRL, 0xf, 0xf, true); }
constexpr int ROR1 = 0x121, ROR2 = 0x122, ROR4 = 0x124, ROR8 = 0x128, QXOR1 = 0xB1;
__device__ __forceinline__ float xrow_sum(float x) {
    auto s = __builtin_amdgcn_permlane16_swap(__float_as_uint(x), __float_as_uint(x), false, false);
    x = __uint_as_float(s[0]) + __uint_as_float(s[1]);
    auto t = __builtin_amdgcn_permlane32_swap(__float_as_uint(x), __float_as_uint(x), false, false);
    return __uint_as_float(t[0]) + __uint_as_float(t[1]);
}
__device__ __forceinline__ float xrow_max(float x) {
    auto s = __builtin_amdgcn_permlane16_swap(__float_as_uint(x), __float_as_uint(x), false, false);
    x = fmaxf(__uint_as_float(s[0]), __uint_as_float(s[1]));
    auto t = __builtin_amdgcn_permlane32_swap(__float_as_uint(x), __float_as_uint(x), false, false);
    return fmaxf(__uint_as_float(t[0]), __uint_as_float(t[1]));
}
__device__ __forceinline__ float rs32(float x, float y) {
    auto t = __builtin_amdgcn_permlane32_swap(__float_as_uint(x), __float_as_uint(y), false, false);
    return __uint_as_float(t[0]) + __uint_as_float(t[1]);
}
__device__ __forceinline__ float rs16(float x, float y) {
    auto t = __builtin_amdgcn_permlane16_swap(__float_as_uint(x), __float_as_uint(y), false, false);
    return __uint_as_float(t[0]) + __uint_as_float(t[1]);
}
__device__ __forceinline__ f32x4 mfma(v4u a, v4u b, f32x4 c) {
    return __builtin_amdgcn_mfma_f32_16x16x32_bf16(__builtin_bit_cast(bf16x8, a), __builtin_bit_cast(bf16x8, b), c, 0, 0, 0);
}
__device__ __forceinline__ v4u ldnt(const void* p) { return __builtin_nontemporal_load((const GAS v4u*)p); }
__device__ __forceinline__ float ex2(float x) { return __builtin_amdgcn_exp2f(x); }
__device__ __forceinline__ float sigmoidf_(float z) { return 1.f / (1.f + __expf(-z)); }
__device__ __forceinline__ int fresh_lane() {
    int l = __builtin_amdgcn_mbcnt_hi(~0u, __builtin_amdgcn_mbcnt_lo(~0u, 0u));
    asm volatile("" : "+v"(l));
    return l;
}
#define SB() __builtin_amdgcn_sched_barrier(0)
__device__ __forceinline__ v4u poll16(const unsigned char* p) {
    v4u r;
    asm volatile("global_load_dwordx4 %0, %1, off sc1\n\ts_waitcnt vmcnt(0)" : "=&v"(r) : "v"(p) : "memory");
    return r;
}
__device__ __forceinline__ void poll16x2(const unsigned char* p, const unsigned char* q, v4u& a, v4u& b) {
    asm volatile("global_load_dwordx4 %0, %2, off sc1\n\tglobal_load_dwordx4 %1, %3, off sc1\n\ts_waitcnt vmcnt(0)" : "=&v"(a), "=&v"(b) : "v"(p), "v"(q) : "memory");
}
__device__ __forceinline__ bool tagok(const v4u& u, u32 tag) { return u[1] == tag && u[3] == tag; }

struct KV { v4u r[8]; };
__device__ __forceinline__ void gemv_block(const v4u* wv, const float (&xv)[4], float (&a)[8]) {
#pragma unroll
    for (int k = 0; k < 8; ++k) a[k] = 0.f;
#pragma unroll
    for (int i = 0; i < 4; ++i)
#pragma unroll
        for (int k = 0; k < 4; ++k) { a[2 * k] = fmaf(xv[i], bflo(wv[i][k]), a[2 * k]); a[2 * k + 1] = fmaf(xv[i], bfhi(wv[i][k]), a[2 * k + 1]); }
#pragma unroll
    for (int k = 0; k < 8; ++k) a[k] = xrow_sum(a[k]);
}
__device__ __forceinline__ u32 tile_off(int n, int w, int cls, u32 tbase) {
    constexpr int m0[8] = {6, 6, 4, 6, 5, 3, 2, 0}, m1[8] = {6, 6, 4, 6, 5, 3, 1, 0}, m2[8] = {6, 6, 4, 6, 6, 4, 4, 0};
    int start = 0, m = 0;
#pragma unroll
    for (int r = 0; r < 8; ++r) { const int mr = cls == 0 ? m0[r] : (cls == 1 ? m1[r] : m2[r]); if (r < n) start += mr; if (r == n) m = mr; }
    return (n < 8 && w <= m) ? (tbase + (u32)(start + w - 1)) * 8192u : OOB;
}
}

#define LD4(R0, R1, R2, R3, VO, RS, O1, O2, O3) asm volatile( \
        "buffer_load_dwordx4 %0, %4, %5, 0 offen nt\n\tbuffer_load_dwordx4 %1, %4, %5, 0 offen offset:" #O1 " nt\n\t" \
        "buffer_load_dwordx4 %2, %4, %5, 0 offen offset:" #O2 " nt\n\tbuffer_load_dwordx4 %3, %4, %5, 0 offen offset:" #O3 " nt" \
        : "=&v"(R0), "=&v"(R1), "=&v"(R2), "=&v"(R3) : "v"(VO), "s"(RS))
#define LD4S(R0, R1, R2, R3, VO, RS, S1, S2, S3) asm volatile( \
        "buffer_load_dwordx4 %0, %4, %5, 0 offen nt\n\tbuffer_load_dwordx4 %1, %4, %5, %6 offen nt\n\t" \
        "buffer_load_dwordx4 %2, %4, %5, %7 offen nt\n\tbuffer_load_dwordx4 %3, %4, %5, %8 offen nt" \
        : "=&v"(R0), "=&v"(R1), "=&v"(R2), "=&v"(R3) : "v"(VO), "s"(RS), "s"(S1), "s"(S2), "s"(S3))
#define LDK4(BUF, H, TO) LD4(BUF.r[4 * (H)], BUF.r[4 * (H) + 1], BUF.r[4 * (H) + 2], BUF.r[4 * (H) + 3], kvo + (TO) + 4096u * (H), rk, 128, 2048, 2176)
#define LDV4(BUF, H, TO) LD4(BUF.r[4 * (H)], BUF.r[4 * (H) + 1], BUF.r[4 * (H) + 2], BUF.r[4 * (H) + 3], vvo + (TO) + 4096u * (H), rv, 1024, 2048, 3072)
#define ISSUE_K(BUF, TO) do { LDK4(BUF, 0, TO); LDK4(BUF, 1, TO); } while (0)
#define ISSUE_V(BUF, TO) do { LDV4(BUF, 0, TO); LDV4(BUF, 1, TO); } while (0)
#define DMA1(LDSA, VO, RS, SO) asm volatile("s_mov_b32 m0, %0\n\ts_nop 0\n\tbuffer_load_dwordx4 %1, %2, %3 offen nt lds" :: "s"(LDSA), "v"(VO), "s"(RS), "s"(SO) : "m0", "memory")
#define DMA_K(LDSA, TO) do { _Pragma("unroll") for (int i_ = 0; i_ < 8; ++i_) DMA1((LDSA) + 1024u * i_, kvo + (TO), rk, (u32)((i_ >> 1) * 2048 + (i_ & 1) * 128)); } while (0)
#define DMA_V(LDSA, TO) do { _Pragma("unroll") for (int i_ = 0; i_ < 8; ++i_) DMA1((LDSA) + 8192u + 1024u * i_, vvo + (TO), rv, (u32)(i_ * 1024)); } while (0)
#define ISSUE_W() do { const int l_ = fresh_lane(); \
                       const u32 woo_ = (u32)(((16 * b + (l_ >> 4)) * D + 8 * (l_ & 15)) * 2); \
                       const u32 wdo_ = has_ffn ? (u32)(((128 * cgf + 16 * ks + (l_ >> 4)) * D + 8 * (l_ & 15)) * 2) : OOB; \
                       for (int c_ = 0; c_ < 8; ++c_) { \
                       _Pragma("unroll") for (int i_ = 0; i_ < 4; ++i_) DMA1(ldsbase + WBASE + 8192u * c_ + 1024u * i_, wdo_, rd, (u32)(8192 * i_ + 256 * c_)); \
                       _Pragma("unroll") for (int i_ = 0; i_ < 4; ++i_) DMA1(ldsbase + WBASE + 8192u * c_ + 4096u + 1024u * i_, woo_, ro, (u32)(8192 * i_ + 256 * c_)); } } while (0)
#define WAIT8(BUF, N) asm volatile("s_waitcnt vmcnt(%8)" : "+v"(BUF.r[0]), "+v"(BUF.r[1]), "+v"(BUF.r[2]), "+v"(BUF.r[3]), "+v"(BUF.r[4]), "+v"(BUF.r[5]), "+v"(BUF.r[6]), "+v"(BUF.r[7]) : "n"(N))

__global__ void __launch_bounds__(NTHR, 2)
fused_decode(const u16* __restrict__ x, const u16* __restrict__ kc, const u16* __restrict__ vc, const u16* __restrict__ wq, const u16* __restrict__ wo,
             const u16* __restrict__ wr, const u16* __restrict__ wg, const u16* __restrict__ wu, const u16* __restrict__ wd, const u16* __restrict__ gw,
             u16* __restrict__ out, unsigned char* ws)
{
    const int tid = threadIdx.x, lane = tid & 63, w = __builtin_amdgcn_readfirstlane(tid >> 6);
    __shared__ __attribute__((aligned(16))) float lds[4 * 4096 + 16384 + 3 * 1024 + 512 + 512 + 2048];
    float* gpart = lds + 4 * 4096 + 16384;
    float* upart = gpart + 1024;
    float* qpart = gpart + 2048;
    float* sm = gpart + 3072;
    u32* qs = (u32*)(gpart + 3072 + 512);
    constexpr u32 WBASE = 4 * 16384;
    constexpr int REC56 = 4 * 4096 + 16384 + 3 * 1024 + 1024;
    float* gsc = sm;
    float* wml = sm + 32;
    float* cw = sm + 160;
    float* cml = sm + 288;
    float* fin = sm + 320;
    volatile LAS u32* qflag = (volatile LAS u32*)(sm + 480);
    volatile LAS u32* gucnt = (volatile LAS u32*)(sm + 481);
    float* gts = sm + 484;
    float* hs = sm + 488;

    const int b = blockIdx.x, kvh = b >> 6, rr = b & 63, G = b >> 3, ks = b & 7;
    const __amdgpu_buffer_rsrc_t rs = __builtin_amdgcn_make_buffer_rsrc((void*)ws, 0, (int)WS_END, 0x00020000);
    const bool has_ffn = (G & 7) != 7;
    const int cgf = kvh * 7 + (G & 7);
    const int lc = lane & 15, lr = lane >> 4;

    const int grow = 128 * ks + 16 * w + lr;
    u32 E, xr[4], xp, gp, rp;
    asm volatile("global_load_dword %0, %8, %9 sc1\n\t"
                 "global_load_ushort %1, %10, %11\n\tglobal_load_ushort %2, %10, %11 offset:8\n\tglobal_load_ushort %3, %10, %11 offset:16\n\tglobal_load_ushort %4, %10, %11 offset:24\n\t"
                 "global_load_dword %5, %12, %11\n\tglobal_load_dword %6, %12, %13\n\tglobal_load_dword %7, %12, %14"
                 : "=&v"(E), "=&v"(xr[0]), "=&v"(xr[1]), "=&v"(xr[2]), "=&v"(xr[3]), "=&v"(xp), "=&v"(gp), "=&v"(rp)
                 : "v"(0u), "s"(ws), "v"((u32)(grow * 2)), "s"(x), "v"((u32)(tid * 4)), "s"(gw), "s"(wr));
    if (tid == 0) { *qflag = 0u; *gucnt = 0u; }
    SB();
    const __amdgpu_buffer_rsrc_t rq = __builtin_amdgcn_make_buffer_rsrc((void*)wq, 0, D * 4096 * 2, 0x00020000);
    const __amdgpu_buffer_rsrc_t rk = __builtin_amdgcn_make_buffer_rsrc((void*)(kc + (size_t)kvh * SK * DH), 0, SK * DH * 2, 0x00020000);
    const __amdgpu_buffer_rsrc_t rv = __builtin_amdgcn_make_buffer_rsrc((void*)(vc + (size_t)kvh * SK * DH), 0, SK * DH * 2, 0x00020000);
    const __amdgpu_buffer_rsrc_t rg = __builtin_amdgcn_make_buffer_rsrc((void*)wg, 0, D * FF * 2, 0x00020000);
    const __amdgpu_buffer_rsrc_t ru = __builtin_amdgcn_make_buffer_rsrc((void*)wu, 0, D * FF * 2, 0x00020000);
    const __amdgpu_buffer_rsrc_t rd = __builtin_amdgcn_make_buffer_rsrc((void*)wd, 0, FF * D * 2, 0x00020000);
    const __amdgpu_buffer_rsrc_t ro = __builtin_amdgcn_make_buffer_rsrc((void*)wo, 0, 4096 * D * 2, 0x00020000);
    const u32 kvo = (u32)((4 * ((lc >> 1) & 1) + (lc >> 2)) * 256 + (lc & 1) * 64 + lr * 16);
    const u32 vvo = (u32)(lane * 16);
    const int cls = !has_ffn ? 2 : (rr < 24 ? 0 : 1);
    const u32 tbase = cls == 0 ? 32u * (u32)rr : (cls == 1 ? 768u + 31u * (u32)(rr - 24) : 1760u + 36u * (u32)(rr - 56));
    KV KA, VA, KB, VB;
    const u32 ldsbase = (u32)(size_t)(LAS void*)lds;

    KV WQ;
    LD4S(WQ.r[0], WQ.r[1], WQ.r[2], WQ.r[3], (u32)((grow * 4096 + 128 * G + 8 * lc) * 2), rq, 4 * 8192, 8 * 8192, 12 * 8192);
    SB();
    __builtin_amdgcn_s_barrier();
    SB();
    const u32 guo = has_ffn ? (u32)((grow * FF + 128 * cgf + 8 * lc) * 2) : OOB;
    {
        const u32 gu0 = w == 0 ? OOB : guo;
        LD4S(KB.r[0], KB.r[1], KB.r[2], KB.r[3], gu0, rg, 4 * FF * 2, 8 * FF * 2, 12 * FF * 2);
        LD4S(KB.r[4], KB.r[5], KB.r[6], KB.r[7], gu0, ru, 4 * FF * 2, 8 * FF * 2, 12 * FF * 2);
    }
    asm volatile("s_waitcnt vmcnt(8)" : "+v"(WQ.r[0]), "+v"(WQ.r[1]), "+v"(WQ.r[2]), "+v"(WQ.r[3]),
                 "+v"(E), "+v"(xr[0]), "+v"(xr[1]), "+v"(xr[2]), "+v"(xr[3]), "+v"(xp), "+v"(gp), "+v"(rp));
    float xv[4];
#pragma unroll
    for (int i = 0; i < 4; ++i) xv[i] = bf2f(xr[i]);
    {
        float a[8];
        gemv_block(&WQ.r[0], xv, a);
        if (lr == 0) { float* dst = qpart + w * 128 + 8 * lc; *(f32x4*)dst = f32x4{a[0], a[1], a[2], a[3]}; *(f32x4*)(dst + 4) = f32x4{a[4], a[5], a[6], a[7]}; }
        float z = rbf(bflo(xp) * bflo(gp)) + rbf(bfhi(xp) * bfhi(gp));
        float r = bflo(xp) * bflo(rp) + bfhi(xp) * bfhi(rp);
        z += dpp<ROR1>(z); z += dpp<ROR2>(z); z += dpp<ROR4>(z); z += dpp<ROR8>(z); z = xrow_sum(z);
        r += dpp<ROR1>(r); r += dpp<ROR2>(r); r += dpp<ROR4>(r); r += dpp<ROR8>(r); r = xrow_sum(r);
        if (lane == 0) { gsc[2 * w] = z; gsc[2 * w + 1] = r; }
    }
    SB();
    __syncthreads();

    const u32 tag = __builtin_amdgcn_readfirstlane(E) + 1u;
    auto gate_up = [&]() {
        if (has_ffn) {
            float ag[8], au[8];
            gemv_block(&KB.r[0], xv, ag);
            gemv_block(&KB.r[4], xv, au);
            if (lr == 0) {
                float* dg = gpart + w * 128 + 8 * lc; float* du = upart + w * 128 + 8 * lc;
                *(f32x4*)dg = f32x4{ag[0], ag[1], ag[2], ag[3]}; *(f32x4*)(dg + 4) = f32x4{ag[4], ag[5], ag[6], ag[7]};
                *(f32x4*)du = f32x4{au[0], au[1], au[2], au[3]}; *(f32x4*)(du + 4) = f32x4{au[4], au[5], au[6], au[7]};
            }
            asm volatile("s_waitcnt lgkmcnt(0)" ::: "memory");
            u32 old = 0;
            if (lane == 0) old = __hip_atomic_fetch_add((LAS u32*)gucnt, 1u, __ATOMIC_RELAXED, __HIP_MEMORY_SCOPE_WORKGROUP);
            if (__builtin_amdgcn_readfirstlane(old) == 7u) {
                float g0 = 0.f, g1 = 0.f, u0 = 0.f, u1 = 0.f;
#pragma unroll
                for (int ww = 0; ww < 8; ++ww) { const float2 vg = *(const float2*)(gpart + ww * 128 + 2 * lane), vu = *(const float2*)(upart + ww * 128 + 2 * lane); g0 += vg.x; g1 += vg.y; u0 += vu.x; u1 += vu.y; }
                v4u ug = {__float_as_uint(g0), tag, __float_as_uint(g1), tag}, uu_ = {__float_as_uint(u0), tag, __float_as_uint(u1), tag};
                __builtin_amdgcn_raw_buffer_store_b128(ug, rs, (int)(WS_GP + (b * 64 + lane) * 16), 0, AUX_SC1);
                __builtin_amdgcn_raw_buffer_store_b128(uu_, rs, (int)(WS_UP + (b * 64 + lane) * 16), 0, AUX_SC1);
            }
        }
    };

    if (w == 0)
        {
            float s0 = 0.f, s1 = 0.f;
#pragma unroll
            for (int ww = 0; ww < 8; ++ww) { const float2 v = *(const float2*)(qpart + ww * 128 + 2 * lane); s0 += v.x; s1 += v.y; }
            v4u u = {__float_as_uint(s0), tag, __float_as_uint(s1), tag};
            __builtin_amdgcn_raw_buffer_store_b128(u, rs, (int)(WS_QP + (b * 64 + lane) * 16), 0, AUX_SC1);
        }
    WAIT8(KB, 0);
    if (w != 0) gate_up();

    constexpr float C2 = 0.08838834764831845f * 1.4426950408889634f;
    const bool uhi_ = (lane >> 3) & 1;
    const u32 psh = uhi_ ? 16u : 0u;
    float m_run = -INFINITY, l_run = 0.f;
    f32x4 acc[8];
#pragma unroll
    for (int d = 0; d < 8; ++d) acc[d] = f32x4{0.f, 0.f, 0.f, 0.f};
    v4u qf[2];
    float sc[4];
    auto qk = [&](const KV& Kb) {
#pragma unroll
        for (int t = 0; t < 4; ++t) {
            f32x4 c = {0.f, 0.f, 0.f, 0.f};
            c = mfma(Kb.r[2 * t], qf[0], c);
            c = mfma(Kb.r[2 * t + 1], qf[1], c);
            const float t0 = c[0] + dpp<ROR8>(c[1]);
            const float t1 = c[3] + dpp<ROR8>(c[2]);
            sc[t] = uhi_ ? t1 : t0;
        }
    };
    auto softmax_pv = [&](const KV& Vb) {
        float mx = fmaxf(fmaxf(sc[0], sc[1]), fmaxf(sc[2], sc[3]));
        mx = fmaxf(mx, dpp<ROR8>(mx));
        mx = xrow_max(mx);
        const float mn = fmaxf(m_run, mx * C2);
        const float alpha = ex2(m_run - mn);
        m_run = mn;
        v4u pf;
        float ps = 0.f;
#pragma unroll
        for (int t = 0; t < 4; ++t) { const float p = ex2(fmaf(sc[t], C2, -mn)); const u32 pb = cvt1(p); ps += bf2f(pb); pf[t] = pb << psh; }
        l_run = fmaf(l_run, alpha, ps);
#pragma unroll
        for (int d = 0; d < 8; ++d) acc[d] *= alpha;
#pragma unroll
        for (int d = 0; d < 8; ++d) {
            v4u a;
#pragma unroll
            for (int q = 0; q < 4; ++q) a[q] = __builtin_amdgcn_perm(Vb.r[2 * q + 1][d >> 1], Vb.r[2 * q][d >> 1], (d & 1) ? 0x07060302u : 0x05040100u);
            acc[d] = mfma(a, pf, acc[d]);
        }
    };

    if (w == 0) {
        const int uo = ((G * 8 + (lane >> 3)) * 64 + ks * 8 + (lane & 7)) * 16;
        v4u qa = poll16(ws + WS_QP + uo);
        u32 spins = 0;
        while (!tagok(qa, tag) && ++spins < SPIN_CAP) qa = poll16(ws + WS_QP + uo);

        float s0 = __uint_as_float(qa[0]), s1 = __uint_as_float(qa[2]);
        s0 += dpp<ROR8>(s0); s1 += dpp<ROR8>(s1); s0 = xrow_sum(s0); s1 = xrow_sum(s1);
        const u32 pr = cvt1(s0) | (cvt1(s1) << 16);
        const u32 nb = dppu<QXOR1>(pr);
        if (lane < 8 && (lane & 1) == 0) {
            v4u uq = {pr, tag, nb, tag};
            __builtin_amdgcn_raw_buffer_store_b128(uq, rs, (int)(WS_QG + (kvh * 256 + rr * 4 + (lane >> 1)) * 16), 0, AUX_SC1);
        }
        const unsigned char* qp = ws + WS_QG + (kvh * 256 + lane) * 16;
        v4u q0, q1u, q2u, q3u;
        spins = 0;
        do {
            asm volatile("global_load_dwordx4 %0, %4, off sc1\n\tglobal_load_dwordx4 %1, %4, off offset:1024 sc1\n\tglobal_load_dwordx4 %2, %4, off offset:2048 sc1\n\tglobal_load_dwordx4 %3, %4, off offset:3072 sc1\n\ts_waitcnt vmcnt(0)"
                         : "=&v"(q0), "=&v"(q1u), "=&v"(q2u), "=&v"(q3u) : "v"(qp) : "memory");
        } while (!(tagok(q0, tag) && tagok(q1u, tag) && tagok(q2u, tag) && tagok(q3u, tag)) && ++spins < SPIN_CAP);
        *(v2u*)(qs + lane * 2) = v2u{q0[0], q0[2]};
        *(v2u*)(qs + (64 + lane) * 2) = v2u{q1u[0], q1u[2]};
        *(v2u*)(qs + (128 + lane) * 2) = v2u{q2u[0], q2u[2]};
        *(v2u*)(qs + (192 + lane) * 2) = v2u{q3u[0], q3u[2]};
        asm volatile("s_waitcnt lgkmcnt(0)" ::: "memory");
        *qflag = 1u;

        LD4S(KB.r[0], KB.r[1], KB.r[2], KB.r[3], guo, rg, 4 * FF * 2, 8 * FF * 2, 12 * FF * 2);
        LD4S(KB.r[4], KB.r[5], KB.r[6], KB.r[7], guo, ru, 4 * FF * 2, 8 * FF * 2, 12 * FF * 2);
        WAIT8(KB, 0);
        gate_up();
        float z = 0.f, r = 0.f;
#pragma unroll
        for (int ww = 0; ww < 8; ++ww) { z += gsc[2 * ww]; r += gsc[2 * ww + 1]; }
        const float g = rbf(sigmoidf_(rbf(z))), sr = rbf(sigmoidf_(rbf(r)));
        const float omg = rbf(1.f - g);
        float h0 = 0.f, h1 = 0.f;
        if (has_ffn) {
            v4u gu, uu;
            spins = 0;
            for (;;) {
                poll16x2(ws + WS_GP + uo, ws + WS_UP + uo, gu, uu);
                if (__all(tagok(gu, tag) && tagok(uu, tag)) || ++spins > SPIN_CAP) break;
                __builtin_amdgcn_s_sleep(8);
            }
            float g0 = __uint_as_float(gu[0]), g1 = __uint_as_float(gu[2]), u0 = __uint_as_float(uu[0]), u1 = __uint_as_float(uu[2]);
            g0 += dpp<ROR8>(g0); g1 += dpp<ROR8>(g1); u0 += dpp<ROR8>(u0); u1 += dpp<ROR8>(u1);
            g0 = rbf(xrow_sum(g0)); g1 = rbf(xrow_sum(g1)); u0 = rbf(xrow_sum(u0)); u1 = rbf(xrow_sum(u1));
            h0 = rbf(rbf(g0 * sigmoidf_(g0)) * u0) * (omg * sr);
            h1 = rbf(rbf(g1 * sigmoidf_(g1)) * u1) * (omg * sr);
        }
        if (lane < 8) *(float2*)(hs + 2 * lane) = make_float2(h0, h1);
        if (lane == 0) { gts[0] = g; gts[1] = omg; }
    } else {
        const u32 t0 = tile_off(0, w, cls, tbase), t1 = tile_off(1, w, cls, tbase), t2 = tile_off(2, w, cls, tbase);
      if (w == 7) {
        ISSUE_W();
        asm volatile("s_waitcnt vmcnt(0)" ::: "memory");
      } else {
        const u32 slot = w <= 4 ? ldsbase + (u32)(w - 1) * 16384u : 0x2F000u;
        ISSUE_K(KA, t0);
        ISSUE_V(VA, t0);
        ISSUE_K(KB, t1);
        ISSUE_V(VB, t1);
        DMA_K(slot, t2);
        DMA_V(slot, t2);
        {
            u32 spins = 0;
            while (*qflag == 0u) { __builtin_amdgcn_s_sleep(1); if (++spins > 64u * SPIN_CAP) break; }

        }
#pragma unroll
        for (int s = 0; s < 2; ++s) qf[s] = *(const v4u*)(qs + (lane & 7) * 64 + 32 * s + 16 * ((lane >> 3) & 1) + 4 * lr);
        u32 ta = tile_off(3, w, cls, tbase);
        WAIT8(KA, 40); qk(KA); SB();
        ISSUE_K(KA, ta);
        WAIT8(VA, 40); softmax_pv(VA); SB();
        ISSUE_V(VA, ta);

        WAIT8(KB, 40); qk(KB); SB();
        WAIT8(VB, 32); softmax_pv(VB); SB();

        u32 tb = tile_off(4, w, cls, tbase);
        asm volatile("s_waitcnt vmcnt(16)" ::: "memory");

        {
            const bool vc_ = t2 != OOB;
            const LAS v4u* sl = (const LAS v4u*)(lds + (w - 1) * 4096) + lane;
            if (vc_) {
#pragma unroll
                for (int i = 0; i < 8; ++i) { KB.r[i] = sl[64 * i]; VB.r[i] = sl[512 + 64 * i]; }
                qk(KB);
            }
            SB();
            ISSUE_K(KB, tb);
            if (vc_) softmax_pv(VB);
            SB();
            ISSUE_V(VB, tb);
        }
        for (int n = 5; ta != OOB; n += 2) {
            WAIT8(KA, 24); qk(KA); SB();
            ta = tile_off(n, w, cls, tbase);
            ISSUE_K(KA, ta);
            WAIT8(VA, 24); softmax_pv(VA); SB();
            ISSUE_V(VA, ta);
            const bool vb = tb != OOB;
            WAIT8(KB, 24);
            if (vb) qk(KB);
            SB();
            tb = tile_off(n + 1, w, cls, tbase);
            ISSUE_K(KB, tb);
            WAIT8(VB, 24);
            if (vb) softmax_pv(VB);
            SB();
            ISSUE_V(VB, tb);
        }
        WAIT8(KA, 0); WAIT8(VA, 0); WAIT8(KB, 0); WAIT8(VB, 0);
      }
    }

    {
    const int lane = fresh_lane(), tid = w * 64 + lane, lc = lane & 15, lr = lane >> 4, ocol = 128 * w + 8 * lc;
    const bool uhi = (lane >> 3) & 1;
    {
        l_run += dpp<ROR8>(l_run); l_run = xrow_sum(l_run);
#pragma unroll
        for (int d = 0; d < 8; ++d)
#pragma unroll
            for (int i = 0; i < 4; ++i) acc[d][i] += dpp<ROR8>(acc[d][i]);
        if (!uhi && w != 0 && w != 7) {
            float* dst = lds + (w <= 4 ? (w - 1) * 4096 : REC56 + (w - 5) * 1024) + (lane & 7) * 128 + 32 * lr;
#pragma unroll
            for (int i = 0; i < 4; ++i) {
                *(f32x4*)(dst + 8 * i) = f32x4{acc[0][i], acc[1][i], acc[2][i], acc[3][i]};
                *(f32x4*)(dst + 8 * i + 4) = f32x4{acc[4][i], acc[5][i], acc[6][i], acc[7][i]};
            }
            if (lr == 0) { wml[(w * 8 + (lane & 7)) * 2] = m_run; wml[(w * 8 + (lane & 7)) * 2 + 1] = l_run; }
        }
    }
    __syncthreads();

    {
        float M = -INFINITY;
#pragma unroll
        for (int ww = 1; ww < 7; ++ww) M = fmaxf(M, wml[(ww * 8 + w) * 2]);
        float L = 0.f, o0 = 0.f, o1 = 0.f;
#pragma unroll
        for (int ww = 1; ww < 7; ++ww) {
            const float f = ex2(wml[(ww * 8 + w) * 2] - M);
            L = fmaf(f, wml[(ww * 8 + w) * 2 + 1], L);
            const float2 v = *(const float2*)(lds + (ww <= 4 ? (ww - 1) * 4096 : REC56 + (ww - 5) * 1024) + w * 128 + 2 * lane);
            o0 = fmaf(f, v.x, o0); o1 = fmaf(f, v.y, o1);
        }
        v4u u = {__float_as_uint(o0), tag, __float_as_uint(o1), tag};
        __builtin_amdgcn_raw_buffer_store_b128(u, rs, (int)(WS_P + ((b * 8 + w) * 64 + lane) * 16), 0, AUX_SC1);
        if (lane == 0) { v4u ml = {__float_as_uint(M), tag, __float_as_uint(L), tag};
            __builtin_amdgcn_raw_buffer_store_b128(ml, rs, (int)(WS_ML + (b * 8 + w) * 16), 0, AUX_SC1); }
    }

    const int hq_l = rr >> 3, d0p = (rr & 7) * 8;
    const int src = kvh * 64 + (tid >> 3);
    const unsigned char* pp = ws + WS_P + ((src * 8 + hq_l) * 64 + d0p + (tid & 7)) * 16;
    const unsigned char* mp = ws + WS_ML + (src * 8 + hq_l) * 16;
    v4u pv, ml;
    asm volatile("global_load_dwordx4 %0, %2, off sc1\n\tglobal_load_dwordx4 %1, %3, off sc1" : "=&v"(pv), "=&v"(ml) : "v"(pp), "v"(mp) : "memory");
    KV W;
    {
        const LAS v4u* wl = (const LAS v4u*)((LAS unsigned char*)lds + WBASE + 8192 * w) + lane;
#pragma unroll
        for (int i = 0; i < 8; ++i) W.r[i] = wl[64 * i];
    }
    const float g = gts[0], omg = gts[1];
    float a[8];
#pragma unroll
    for (int k = 0; k < 8; ++k) a[k] = 0.f;
#pragma unroll
    for (int i = 0; i < 4; ++i) {
        const float ch = hs[4 * i + lr];
#pragma unroll
        for (int k = 0; k < 4; ++k) { a[2 * k] = fmaf(ch, bflo(W.r[i][k]), a[2 * k]); a[2 * k + 1] = fmaf(ch, bfhi(W.r[i][k]), a[2 * k + 1]); }
    }
    float wf[4][8];
#pragma unroll
    for (int i = 0; i < 4; ++i)
#pragma unroll
        for (int k = 0; k < 4; ++k) { wf[i][2 * k] = bflo(W.r[4 + i][k]); wf[i][2 * k + 1] = bfhi(W.r[4 + i][k]); }

    {
        asm volatile("s_waitcnt vmcnt(0)" : "+v"(pv), "+v"(ml));
        u32 spins = 0;
        while (!(tagok(pv, tag) && tagok(ml, tag)) && ++spins < SPIN_CAP) poll16x2(pp, mp, pv, ml);
        const float ms = __uint_as_float(ml[0]), ls = __uint_as_float(ml[2]);
        float Mw = fmaxf(ms, dpp<ROR8>(ms)); Mw = xrow_max(Mw);
        const float e = ex2(ms - Mw);
        float L = e * ls, o0 = e * __uint_as_float(pv[0]), o1 = e * __uint_as_float(pv[2]);
        L += dpp<ROR8>(L); o0 += dpp<ROR8>(o0); o1 += dpp<ROR8>(o1);
        L = xrow_sum(L); o0 = xrow_sum(o0); o1 = xrow_sum(o1);
        if (lane < 8) *(float2*)(cw + w * 16 + 2 * lane) = make_float2(o0, o1);
        if (lane == 0) { cml[2 * w] = Mw; cml[2 * w + 1] = L; }
    }
    __syncthreads();

    {
        float M = -INFINITY;
#pragma unroll
        for (int ww = 0; ww < 8; ++ww) M = fmaxf(M, cml[2 * ww]);
        float L = 0.f, o = 0.f;
#pragma unroll
        for (int ww = 0; ww < 8; ++ww) { const float f = ex2(cml[2 * ww] - M); L = fmaf(f, cml[2 * ww + 1], L); o = fmaf(f, cw[ww * 16 + lc], o); }
        const float ov = rbf(o / L) * g;
#pragma unroll
        for (int i = 0; i < 4; ++i) {
            const float co = __shfl(ov, 4 * i + lr);
#pragma unroll
            for (int k = 0; k < 8; ++k) a[k] = fmaf(co, wf[i][k], a[k]);
        }
        const float b0 = rs32(a[0], a[4]), b1 = rs32(a[1], a[5]), b2 = rs32(a[2], a[6]), b3 = rs32(a[3], a[7]);
        const float v0 = rs16(b0, b2), v1 = rs16(b1, b3);
        v4u u = {__float_as_uint(v0), tag, __float_as_uint(v1), tag};
        __builtin_amdgcn_raw_buffer_store_b128(u, rs, (int)(WS_OUTP + (b * 512 + (ocol >> 1) + lr) * 16), 0, AUX_SC1);

        if (b < 64) {
            const unsigned char* fp = ws + WS_OUTP + ((tid >> 3) * 512 + 8 * b + (tid & 7)) * 16;
            const u32 xo = x[16 * b + (tid & 15)];
            v4u f0, f1, f2, f3;
            u32 spins = 0;
            do {
                asm volatile("global_load_dwordx4 %0, %4, off sc1\n\tglobal_load_dwordx4 %1, %5, off sc1\n\tglobal_load_dwordx4 %2, %6, off sc1\n\tglobal_load_dwordx4 %3, %7, off sc1\n\ts_waitcnt vmcnt(0)"
                             : "=&v"(f0), "=&v"(f1), "=&v"(f2), "=&v"(f3) : "v"(fp), "v"(fp + 64 * 8192), "v"(fp + 128 * 8192), "v"(fp + 192 * 8192) : "memory");
            } while (!(tagok(f0, tag) && tagok(f1, tag) && tagok(f2, tag) && tagok(f3, tag)) && ++spins < SPIN_CAP);

            float s0 = (__uint_as_float(f0[0]) + __uint_as_float(f1[0])) + (__uint_as_float(f2[0]) + __uint_as_float(f3[0]));
            float s1 = (__uint_as_float(f0[2]) + __uint_as_float(f1[2])) + (__uint_as_float(f2[2]) + __uint_as_float(f3[2]));
            s0 += dpp<ROR8>(s0); s1 += dpp<ROR8>(s1); s0 = xrow_sum(s0); s1 = xrow_sum(s1);
            if (lane < 8) *(float2*)(fin + w * 16 + 2 * lane) = make_float2(s0, s1);
            __syncthreads();
            if (tid < 16) {
                float sum = 0.f;
#pragma unroll
                for (int ww = 0; ww < 8; ++ww) sum += fin[ww * 16 + tid];
                out[16 * b + tid] = (u16)cvt1(sum + omg * bf2f(xo));
            }
            if (b == 0 && tid == 0) __hip_atomic_store((GAS u32*)(ws + WS_EPOCH), tag, __ATOMIC_RELAXED, __HIP_MEMORY_SCOPE_AGENT);
        }
    }
    }

}

extern "C" void kernel_launch(void* const* d_in, const int* in_sizes, int n_in, void* d_out, int out_size, void* d_ws, size_t ws_size, hipStream_t stream) {
    const u16* x = (const u16*)d_in[0]; const u16* kc = (const u16*)d_in[1]; const u16* vc = (const u16*)d_in[2];
    const u16* wq = (const u16*)d_in[3]; const u16* wo = (const u16*)d_in[4];
    const u16* wr = (const u16*)d_in[6]; const u16* wg = (const u16*)d_in[7]; const u16* wu = (const u16*)d_in[8]; const u16* wd = (const u16*)d_in[9]; const u16* gw = (const u16*)d_in[10];
    fused_decode<<<NWG, NTHR, 0, stream>>>(x, kc, vc, wq, wo, wr, wg, wu, wd, gw, (u16*)d_out, (unsigned char*)d_ws);
}
```
